```python
import torch

def _softmax(x, axis=-1):
    return torch.softmax(x, dim=axis)
def _gelu(x):
    return torch.nn.functional.gelu(x, approximate="tanh")
def _silu(x):
    return torch.nn.functional.silu(x)
def _sigmoid(x):
    return torch.sigmoid(x)
def _layernorm(x, axis=-1, eps=1e-5):
    m = x.mean(dim=axis, keepdim=True)
    v = x.var(dim=axis, keepdim=True, unbiased=False)
    return (x - m) / torch.sqrt(v + eps)
def _scatter_add(tgt, idx, src, axis=0):
    return tgt.clone().index_add_(axis, idx, src)
def _segment_sum(src, seg, n):
    out = torch.zeros((n,) + src.shape[1:], dtype=src.dtype)
    return out.index_add_(0, seg, src)
def _pad(x, pw):
    flat = tuple(v for pair in reversed(pw) for v in pair)
    return torch.nn.functional.pad(x, flat)

kernel_name = 'interleave_layers__ssm_conv_s4__swiglu_ffn__prompt'

def setup_inputs():

    _w = (lambda a: (lambda t: t.to(torch.bfloat16) if t.is_floating_point() else t)(torch.as_tensor(a)))
    def _A_setup():
        B, L, D, N = 1, 2048, 512, 128
        A_re = -(lambda _x: torch.exp(torch.as_tensor(_x)))((lambda *s: torch.randn(*s, dtype=torch.bfloat16) * 1.6)(D, N))
        A_im = torch.pi * (lambda *s: torch.randn(*s, dtype=torch.bfloat16) * 0.2)(D, N)
        B_re = (lambda *s: torch.randn(*s, dtype=torch.bfloat16) * 0.2)(D, N)
        B_im = (lambda *s: torch.randn(*s, dtype=torch.bfloat16) * 0.2)(D, N)
        C_re = (lambda *s: torch.randn(*s, dtype=torch.bfloat16) * 0.2)(D, N)
        C_im = (lambda *s: torch.randn(*s, dtype=torch.bfloat16) * 0.2)(D, N)
        log_dt = (lambda *s: torch.randn(*s, dtype=torch.bfloat16) * 0.2)(D) * 0.5 - 4.0
        Dskip = (lambda *s: torch.randn(*s, dtype=torch.bfloat16) * 0.2)(D)
        return {
            "u": _w((lambda *s: torch.randn(*s, dtype=torch.bfloat16) * 0.05)(B, L, D)),
            "A_re": _w(A_re),
            "A_im": _w(A_im),
            "B_re": _w(B_re),
            "B_im": _w(B_im),
            "C_re": _w(C_re),
            "C_im": _w(C_im),
            "log_dt": _w(log_dt),
            "Dskip": _w(Dskip),
        }
    _A_in = _A_setup()

    def _B_setup():
        B, S, D, F = 1, 2048, 512, 1376
        return {
            "x": _w((lambda *s: torch.randn(*s, dtype=torch.bfloat16) * 0.05)(B, S, D)),
            "w_gate": _w((lambda *s: torch.randn(*s, dtype=torch.bfloat16) * 0.05)(D, F)),
            "w_up": _w((lambda *s: torch.randn(*s, dtype=torch.bfloat16) * 0.05)(D, F)),
            "w_down": _w((lambda *s: torch.randn(*s, dtype=torch.bfloat16) * 0.05)(F, D)),
        }
    _B_in = _B_setup()

    return {
        "A_u": _A_in["u"],
        "A_A_re": _A_in["A_re"],
        "A_A_im": _A_in["A_im"],
        "A_B_re": _A_in["B_re"],
        "A_B_im": _A_in["B_im"],
        "A_C_re": _A_in["C_re"],
        "A_C_im": _A_in["C_im"],
        "A_log_dt": _A_in["log_dt"],
        "A_Dskip": _A_in["Dskip"],
        "B_x": _B_in["x"],
        "B_w_gate": _B_in["w_gate"],
        "B_w_up": _B_in["w_up"],
        "B_w_down": _B_in["w_down"],
    }

def reference(A_u, A_A_re, A_A_im, A_B_re, A_B_im, A_C_re, A_C_im, A_log_dt, A_Dskip, B_x, B_w_gate, B_w_up, B_w_down):
    def _A_ref(u, A_re, A_im, B_re, B_im, C_re, C_im, log_dt, Dskip):
        Bsz, L, D = u.shape
        dt = torch.exp(log_dt)[:, None]
        A = A_re + 1j * A_im
        Bc = B_re + 1j * B_im
        Cc = C_re + 1j * C_im
        logdA = dt * A
        dB = dt * Bc
        l = (lambda x, dt: x.to(dt))(torch.arange(L), A_re.dtype)
        P = torch.exp(logdA[:, :, None] * l[None, None, :])
        K = torch.einsum("dn,dnl,dn->dl", Cc, P, dB).real
        L2 = 2 * L
        Kp = _pad(K, ((0, 0), (0, L)))
        up = (lambda x, *d: x.permute(*d))(u, 0, 2, 1)
        up = _pad(up, ((0, 0), (0, 0), (0, L)))
        Kf = (lambda x, axes=(-2,-1): torch.fft.rfft2(x.float(), dim=axes))(Kp[:, None, :], axes=(-2, -1))
        Uf = (lambda x, axes=(-2,-1): torch.fft.rfft2(x.float(), dim=axes))(up[:, :, None, :], axes=(-2, -1))
        Yf = Uf * Kf[None, :, :, :]
        y = (lambda x, s, axes=(-2,-1): torch.fft.irfft2(x, s=s, dim=axes).to(torch.bfloat16))(Yf, (1, L2), axes=(-2, -1))[:, :, 0, :L]
        y = (lambda x, *d: x.permute(*d))(y, 0, 2, 1) + u * Dskip[None, None, :]
        return y

    def _B_ref(x, w_gate, w_up, w_down):
        g = _silu(x @ w_gate)
        u = x @ w_up
        return (g * u) @ w_down

    x = A_u
    x = x + _A_ref(_layernorm(x, axis=-1), A_A_re, A_A_im, A_B_re, A_B_im, A_C_re, A_C_im, A_log_dt, A_Dskip)
    x = x + _B_ref(_layernorm(x, axis=-1), B_w_gate, B_w_up, B_w_down)
    x = x + _A_ref(_layernorm(x, axis=-1), A_A_re, A_A_im, A_B_re, A_B_im, A_C_re, A_C_im, A_log_dt, A_Dskip)
    x = x + _B_ref(_layernorm(x, axis=-1), B_w_gate, B_w_up, B_w_down)
    return x
```

```cpp
#include <hip/hip_runtime.h>
#include <hip/hip_bf16.h>
#include <stdint.h>
typedef unsigned short u16;
__device__ __forceinline__ float bf2f(u16 v){ return __uint_as_float(((unsigned)v)<<16); }
__device__ __forceinline__ u16 f2bf(float f){ __hip_bfloat16 b = __float2bfloat16(f); return *(u16*)&b; }
__device__ __forceinline__ float rbf(float f){ return bf2f(f2bf(f)); }
constexpr int L=2048, D=512, N=128, F=1376;

__global__ void kgen(const u16* Are,const u16* Aim,const u16* Bre,const u16* Bim,const u16* Cre,const u16* Cim,const u16* logdt,float* K){
  int l = blockIdx.x*256+threadIdx.x; int d = blockIdx.y;
  float dt = rbf(expf(bf2f(logdt[d])));
  float lam = (float)l;
  float acc=0;
  for(int n=0;n<N;n++){
    float zr = dt*bf2f(Are[d*N+n]), zi = dt*bf2f(Aim[d*N+n]);
    float br = dt*bf2f(Bre[d*N+n]), bi = dt*bf2f(Bim[d*N+n]);
    float cr = bf2f(Cre[d*N+n]), ci = bf2f(Cim[d*N+n]);
    float wr = cr*br-ci*bi, wi = cr*bi+ci*br;
    float e = expf(zr*lam); float s,c; sincosf(zi*lam,&s,&c);
    acc += e*(wr*c - wi*s);
  }
  K[d*L+l]=acc;
}
__global__ void ln(const u16* x,u16* u){
  int t=blockIdx.x; int tid=threadIdx.x;
  __shared__ float red[256];
  float a=bf2f(x[t*D+tid]), b=bf2f(x[t*D+256+tid]);
  red[tid]=a+b; __syncthreads();
  for(int s=128;s>0;s>>=1){ if(tid<s) red[tid]+=red[tid+s]; __syncthreads(); }
  float mean=red[0]/D; __syncthreads();
  red[tid]=(a-mean)*(a-mean)+(b-mean)*(b-mean); __syncthreads();
  for(int s=128;s>0;s>>=1){ if(tid<s) red[tid]+=red[tid+s]; __syncthreads(); }
  float var=red[0]/D;
  float m=rbf(mean); float v=rbf(var); float sd=rbf(sqrtf(rbf(v+1e-5f)));
  u[t*D+tid]=f2bf(rbf(a-m)/sd); u[t*D+256+tid]=f2bf(rbf(b-m)/sd);
}
__global__ void conv(const u16* x,const u16* u,const float* K,const u16* Dskip,u16* out){
  int d=blockIdx.x*64+(threadIdx.x&63); int t=blockIdx.y*4+(threadIdx.x>>6);
  float acc=0;
  for(int j=0;j<=t;j++) acc += K[d*L+j]*bf2f(u[(t-j)*D+d]);
  float y=rbf(acc);
  float s=rbf(bf2f(u[t*D+d])*bf2f(Dskip[d]));
  float yy=rbf(y+s);
  out[t*D+d]=f2bf(bf2f(x[t*D+d])+yy);
}
__global__ void gemm1(const u16* u,const u16* wg,const u16* wu,u16* h){
  int f=blockIdx.x*32+(threadIdx.x&31); int t=blockIdx.y*8+(threadIdx.x>>5);
  float g=0,up=0;
  for(int k=0;k<D;k++){ float a=bf2f(u[t*D+k]); g+=a*bf2f(wg[k*F+f]); up+=a*bf2f(wu[k*F+f]); }
  g=rbf(g); up=rbf(up);
  float s=rbf(g/(1.f+expf(-g)));
  h[t*F+f]=f2bf(s*up);
}
__global__ void gemm2(const u16* h,const u16* wd,const u16* x,u16* out){
  int d=blockIdx.x*32+(threadIdx.x&31); int t=blockIdx.y*8+(threadIdx.x>>5);
  float a=0;
  for(int k=0;k<F;k++) a+=bf2f(h[t*F+k])*bf2f(wd[k*D+d]);
  out[t*D+d]=f2bf(bf2f(x[t*D+d])+rbf(a));
}
extern "C" void kernel_launch(void* const* d_in, const int* in_sizes, int n_in,
                              void* d_out, int out_size, void* d_ws, size_t ws_size,
                              hipStream_t stream) {
  const u16* x0=(const u16*)d_in[0];
  char* ws=(char*)d_ws;
  float* K=(float*)ws; u16* u=(u16*)(ws+(8<<20)); u16* x1=(u16*)(ws+(12<<20)); u16* x2=(u16*)(ws+(16<<20)); u16* h=(u16*)(ws+(20<<20));
  kgen<<<dim3(L/256,D),256,0,stream>>>((const u16*)d_in[1],(const u16*)d_in[2],(const u16*)d_in[3],(const u16*)d_in[4],(const u16*)d_in[5],(const u16*)d_in[6],(const u16*)d_in[7],K);
  const u16* xin=x0;
  for(int layer=0;layer<2;layer++){
    ln<<<L,256,0,stream>>>(xin,u);
    conv<<<dim3(D/64,L/4),256,0,stream>>>(xin,u,K,(const u16*)d_in[8],x1);
    ln<<<L,256,0,stream>>>(x1,u);
    gemm1<<<dim3(F/32,L/8),256,0,stream>>>(u,(const u16*)d_in[10],(const u16*)d_in[11],h);
    u16* o = layer==0? x2 : (u16*)d_out;
    gemm2<<<dim3(D/32,L/8),256,0,stream>>>(h,(const u16*)d_in[12],x1,o);
    xin=x2;
  }
}
```

```cpp
#include <hip/hip_runtime.h>
#include <stdint.h>

typedef unsigned short u16;
typedef unsigned int u32;
typedef unsigned long long u64;
typedef float f32x2 __attribute__((ext_vector_type(2)));
typedef float f32x4 __attribute__((ext_vector_type(4)));
typedef double f64x2 __attribute__((ext_vector_type(2)));
typedef u32 u32x2 __attribute__((ext_vector_type(2)));
typedef u32 u32x4 __attribute__((ext_vector_type(4)));
typedef short s16x4 __attribute__((ext_vector_type(4)));
typedef __bf16 bf16x2 __attribute__((ext_vector_type(2)));
typedef __bf16 bf16x8 __attribute__((ext_vector_type(8)));
typedef _Float16 f16x2 __attribute__((ext_vector_type(2)));
typedef _Float16 f16x8 __attribute__((ext_vector_type(8)));
#define LAS __attribute__((address_space(3)))
#define GAS __attribute__((address_space(1)))
#define DEVI __device__ __forceinline__
#define RLX_AGENT __ATOMIC_RELAXED, __HIP_MEMORY_SCOPE_AGENT

constexpr int L_ = 2048, D_ = 512, N_ = 128, F_ = 1376;
constexpr int NT1 = 172;
constexpr int KS2 = 43;

constexpr size_t WS_KR = 0;
constexpr size_t WS_KSC = WS_KR + (size_t)D_ * L_ * 2;
constexpr size_t WS_STAT = WS_KSC + 4096;
constexpr size_t WS_GRAN = WS_STAT + 2 * 32768;
constexpr size_t WS_CS = WS_GRAN + 2 * 16384;
constexpr size_t WS_XQ0 = WS_CS + 65536;
constexpr size_t WS_XQ2 = WS_XQ0 + (2u << 20);
constexpr size_t WS_XT1 = WS_XQ2 + (2u << 20);
constexpr size_t WS_XT3 = WS_XT1 + (2u << 20);
constexpr size_t WS_W1P = WS_XT3 + (2u << 20);
constexpr size_t WS_W2P = WS_W1P + (size_t)60 * 16 * 3 * 1024;
constexpr size_t WS_H = WS_W2P + (size_t)8 * KS2 * 4 * 1024;
constexpr size_t WS_END = WS_H + (size_t)128 * NT1 * 256;
constexpr u32 GTAG = 0x5a5ac3c3u;

DEVI float bf_lo(u32 v) { return __uint_as_float(v << 16); }
DEVI float bf_hi(u32 v) { return __uint_as_float(v & 0xffff0000u); }
DEVI u32 pk_bf16(float a, float b) { f32x2 v = {a, b}; return __builtin_bit_cast(u32, __builtin_convertvector(v, bf16x2)); }
DEVI u32 pk_f16(float a, float b) { f32x2 v = {a, b}; return __builtin_bit_cast(u32, __builtin_convertvector(v, f16x2)); }
DEVI float rbf(float a) { return bf_lo(pk_bf16(a, a)); }
DEVI f32x4 mfma_bf16(u32x4 a, u32x4 b, f32x4 c) {
  return __builtin_amdgcn_mfma_f32_16x16x32_bf16(__builtin_bit_cast(bf16x8, a), __builtin_bit_cast(bf16x8, b), c, 0, 0, 0);
}
DEVI f32x4 mfma_f16(u32x4 a, u32x4 b, f32x4 c) {
  return __builtin_amdgcn_mfma_f32_16x16x32_f16(__builtin_bit_cast(f16x8, a), __builtin_bit_cast(f16x8, b), c, 0, 0, 0);
}
DEVI u32x2 tr_read(LAS char* p) { return __builtin_bit_cast(u32x2, __builtin_amdgcn_ds_read_tr16_b64_v4i16((LAS s16x4*)p)); }
DEVI float wave_max(float v) {
#pragma unroll
  for (int o = 32; o > 0; o >>= 1) v = fmaxf(v, __shfl_xor(v, o));
  return v;
}
DEVI f32x2 cmul(f32x2 a, f32x2 b) { return f32x2{a.x * b.x - a.y * b.y, a.x * b.y + a.y * b.x}; }
DEVI f32x2 cexp_m(float zr, float zp, float m) {
  float s, c;
  sincospif(m * zp, &s, &c);
  float e = __builtin_amdgcn_exp2f(m * zr * 1.44269504088896341f);
  return f32x2{e * c, e * s};
}
DEVI int lam_idx(u32 bf16bits, int l) { return l < 128 ? l : (int)bf16bits - 0x4300 + 128; }

struct P0 {
  const u16 *x0, *are, *aim, *bre, *bim, *cre, *cim, *logdt, *wg, *wu, *wd;
  char* ws;
};

constexpr int K0_LDS = 4 * 32 * 528 + 2 * 648 * 4 + 64;
__global__ void __launch_bounds__(512) k_prep(P0 p) {
  extern __shared__ __attribute__((aligned(16))) char smem_[];
  LAS char* sm = (LAS char*)smem_;
  const int tid = threadIdx.x, lane = tid & 63, w = __builtin_amdgcn_readfirstlane(tid >> 6);
  int b = blockIdx.x;
  if (b < 256) {
    const int cp = ((b >> 4) * 8 + (b & 7)) * 2 + ((b >> 3) & 1);
    const int c = w >> 2, role = w & 3, d = cp * 2 + c;
    LAS char* Wt = sm + c * (32 * 528);
    LAS char* Vt = sm + (2 + c) * (32 * 528);
    LAS float* T = (LAS float*)(sm + 4 * 32 * 528);
    LAS float* misc = T + 2 * 648;
    const float S = 16384.f;
    const int pi = d * N_ + 2 * lane;
    const u32 are = *(const u32*)(p.are + pi), aim = *(const u32*)(p.aim + pi);
    u32 bre = 0, bim = 0, cre = 0, cim = 0;
    if (role >= 2) {
      bre = *(const u32*)(p.bre + pi); bim = *(const u32*)(p.bim + pi);
      cre = *(const u32*)(p.cre + pi); cim = *(const u32*)(p.cim + pi);
    }
    const float dt = rbf(expf(bf_lo(p.logdt[d])));
    float zr[2] = {dt * bf_lo(are), dt * bf_hi(are)};
    float zp[2] = {dt * bf_lo(aim) * 0.318309886183790672f, dt * bf_hi(aim) * 0.318309886183790672f};
    const float mu = wave_max(fmaxf(zr[0], zr[1]));
    zr[0] -= mu; zr[1] -= mu;
    if (role < 2) {
      f32x2 st[2], cur[2];
#pragma unroll
      for (int i = 0; i < 2; i++) {
        st[i] = cexp_m(zr[i], zp[i], 8.f);
        cur[i] = f32x2{S, 0.f};
        if (role == 1) cur[i] = cexp_m(zr[i], zp[i], 128.f) * S;
      }
      LAS char* dst = Vt + role * 16 * 528 + lane * 8;
#pragma unroll
      for (int j = 0; j < 16; j++) {
        *(LAS u32x2*)(dst + j * 528) = u32x2{pk_f16(cur[0].x, -cur[0].y), pk_f16(cur[1].x, -cur[1].y)};
        if (j < 15) { cur[0] = cmul(cur[0], st[0]); cur[1] = cmul(cur[1], st[1]); }
      }
    } else {
      f32x2 cc[2];
      cc[0] = cmul(f32x2{bf_lo(cre), bf_lo(cim)}, f32x2{dt * bf_lo(bre), dt * bf_lo(bim)});
      cc[1] = cmul(f32x2{bf_hi(cre), bf_hi(cim)}, f32x2{dt * bf_hi(bre), dt * bf_hi(bim)});
      float cm = wave_max(fmaxf(fmaxf(fabsf(cc[0].x), fabsf(cc[0].y)), fmaxf(fabsf(cc[1].x), fabsf(cc[1].y))));
      cm = fmaxf(cm, 1e-30f);
      const float sc = S / cm;
      cc[0] *= sc; cc[1] *= sc;
      if (role == 2 && lane == 0) { misc[c * 2] = cm / (S * S); misc[c * 2 + 1] = mu; }
      f32x2 e256[2] = {cexp_m(zr[0], zp[0], 256.f), cexp_m(zr[1], zp[1], 256.f)};
      const float m2 = role == 2 ? 1.f : 4.f;
      f32x2 es[2] = {cexp_m(zr[0], zp[0], m2), cexp_m(zr[1], zp[1], m2)};
      LAS char* dst = Wt + lane * 8;
#define PUTW(row, v0, v1) *(LAS u32x2*)(dst + (row) * 528) = u32x2{pk_f16((v0).x, (v0).y), pk_f16((v1).x, (v1).y)}
      if (role == 2) {
        f32x2 c0 = cc[0], c1 = cc[1];
#pragma unroll
        for (int j = 0; j < 8; j++) {
          PUTW(j, c0, c1);
          if (j < 7) { c0 = cmul(c0, es[0]); c1 = cmul(c1, es[1]); }
        }
        f32x2 e2[2] = {cmul(es[0], es[0]), cmul(es[1], es[1])};
        c0 = cmul(cc[0], e256[0]); c1 = cmul(cc[1], e256[1]);
#pragma unroll
        for (int j = 0; j < 4; j++) {
          PUTW(8 + j, c0, c1);
          if (j < 3) { c0 = cmul(c0, e2[0]); c1 = cmul(c1, e2[1]); }
        }
      } else {
        f32x2 c0 = cmul(cc[0], e256[0]), c1 = cmul(cc[1], e256[1]);
#pragma unroll
        for (int j = 0; j < 7; j++) {
          c0 = cmul(c0, e256[0]); c1 = cmul(c1, e256[1]);
          if (j < 2) {
            PUTW(12 + 2 * j, c0, c1);
            f32x2 d0 = cmul(c0, es[0]), d1 = cmul(c1, es[1]);
            PUTW(13 + 2 * j, d0, d1);
          } else {
            PUTW(14 + j, c0, c1);
          }
        }
      }
#undef PUTW
    }
    __syncthreads();
    {
      const int rt = role >> 1, ct = role & 1, r = lane & 15, q = lane >> 4;
      LAS char* pa = Wt + (16 * rt + r) * 528 + q * 16;
      LAS char* pb = Vt + (16 * ct + r) * 528 + q * 16;
      f32x4 acc = {0.f, 0.f, 0.f, 0.f};
#pragma unroll
      for (int s = 0; s < 8; s++) acc = mfma_f16(*(LAS u32x4*)(pa + s * 64), *(LAS u32x4*)(pb + s * 64), acc);
      const float back = misc[c * 2], mu2 = misc[c * 2 + 1] * 1.44269504088896341f;
      const int col = 16 * ct + r;
      float mx = 0.f;
#pragma unroll
      for (int j = 0; j < 4; j++) {
        const int row = 16 * rt + 4 * q + j;
        int al = row < 8 ? row : row < 12 ? 256 + 2 * (row - 8) : row < 16 ? 512 + 256 * ((row - 12) >> 1) + 4 * (row & 1) : 1024 + 256 * (row - 16);
        const int lam = al + 8 * col;
        const bool ok = row < 20 || (row == 20 && col == 0);
        const float lf = (float)lam;
        const float v = acc[j] * back * __builtin_amdgcn_exp2f(mu2 * lf);
        if (ok) {
          T[c * 648 + lam_idx(__float_as_uint(lf) >> 16, lam)] = v;
          mx = fmaxf(mx, fabsf(v));
        }
      }
      mx = wave_max(mx);
      if (lane == 0) misc[4 + w] = mx;
    }
    __syncthreads();
    {
      const int c2 = tid >> 8, ch = tid & 255;
      float km = fmaxf(fmaxf(misc[4 + 4 * c2], misc[5 + 4 * c2]), fmaxf(misc[6 + 4 * c2], misc[7 + 4 * c2]));
      km = fmaxf(km, 1e-30f);
      const float sc = S / km;
      u32x4 o;
#pragma unroll
      for (int e = 0; e < 4; e++) {
        const int l0 = 2047 - (8 * ch + 2 * e), l1 = l0 - 1;
        const u32 bb = pk_bf16((float)l0, (float)l1);
        const float v0 = T[c2 * 648 + lam_idx(bb & 0xffffu, l0)], v1 = T[c2 * 648 + lam_idx(bb >> 16, l1)];
        o[e] = pk_f16(v0 * sc, v1 * sc);
      }
      const int dd = cp * 2 + c2;
      *(u32x4*)(p.ws + WS_KR + (size_t)dd * 4096 + ch * 16) = o;
      if (ch == 0) ((float*)(p.ws + WS_KSC))[dd] = km / S;
    }
    return;
  }
  b -= 256;
  if (b < 128) {
    const int r = tid & 15, cg = tid >> 4, t = b * 16 + r;
    const u32x4* src = (const u32x4*)(p.x0 + (size_t)t * D_ + cg * 16);
    const u32x4 v0 = src[0], v1 = src[1];
    char* dst = p.ws + WS_XQ0 + ((size_t)(cg * 4) * L_ + t) * 8;
    *(u32x2*)(dst) = u32x2{v0.x, v0.y};
    *(u32x2*)(dst + L_ * 8) = u32x2{v0.z, v0.w};
    *(u32x2*)(dst + 2 * L_ * 8) = u32x2{v1.x, v1.y};
    *(u32x2*)(dst + 3 * L_ * 8) = u32x2{v1.z, v1.w};
    float s = 0.f, ss = 0.f;
#pragma unroll
    for (int i = 0; i < 4; i++) {
      float a = bf_lo(v0[i]), bq = bf_hi(v0[i]), cq = bf_lo(v1[i]), dq = bf_hi(v1[i]);
      s += (a + bq) + (cq + dq);
      ss += (a * a + bq * bq) + (cq * cq + dq * dq);
    }
    s += __shfl_xor(s, 16); ss += __shfl_xor(ss, 16);
    s += __shfl_xor(s, 32); ss += __shfl_xor(ss, 32);
    LAS float* red = (LAS float*)sm;
    if (lane < 16) { red[(w * 16 + lane) * 2] = s; red[(w * 16 + lane) * 2 + 1] = ss; }
    __syncthreads();
    if (tid < 32) {
      const int rr = tid & 15, which = tid >> 4;
      double a = 0.0;
#pragma unroll
      for (int i = 0; i < 8; i++) a += (double)red[(i * 16 + rr) * 2 + which];
      double* st = (double*)(p.ws + WS_STAT);
      st[which * 2048 + b * 16 + rr] = a;
      st[4096 + which * 2048 + b * 16 + rr] = 0.0;
      ((u64*)(p.ws + WS_GRAN))[which * 2048 + b * 16 + rr] = 0ull;
    }
    return;
  }
  b -= 128;
  if (b < 88) {
    const int fb = b >> 2, kq = b & 3;
    const int oct = tid & 7, tau = fb * 8 + oct;
#pragma unroll
    for (int ps = 0; ps < 2; ps++) {
      const int r = ps * 64 + (tid >> 3);
      u32x4 g = {0, 0, 0, 0}, u = {0, 0, 0, 0};
      if (tau < NT1) {
        g = *(const u32x4*)(p.wg + (size_t)(kq * 128 + r) * F_ + tau * 8);
        u = *(const u32x4*)(p.wu + (size_t)(kq * 128 + r) * F_ + tau * 8);
      }
      LAS char* dst = sm + r * 288 + oct * 32;
      *(LAS u32x4*)dst = u32x4{g.x, u.x, g.y, u.y};
      *(LAS u32x4*)(dst + 16) = u32x4{g.z, u.z, g.w, u.w};
    }
    __syncthreads();
    const int tw = fb * 8 + w;
    const int g4 = lane >> 4, q = (lane & 15) >> 2, pp = lane & 3;
    f32x4 cs = {0.f, 0.f, 0.f, 0.f};
    const u32x4 ones = {0x3f803f80u, 0x3f803f80u, 0x3f803f80u, 0x3f803f80u};
    char* dst = p.ws + WS_W1P + ((size_t)(tw / 3) * 48 + (tw % 3)) * 1024 + lane * 16;
#pragma unroll
    for (int k = 0; k < 4; k++) {
      LAS char* a = sm + (32 * k + 8 * g4 + q) * 288 + w * 32 + pp * 8;
      const u32x2 lo = tr_read(a), hi = tr_read(a + 4 * 288);
      const u32x4 fr = {lo.x, lo.y, hi.x, hi.y};
      cs = mfma_bf16(fr, ones, cs);
      if (tw < NT1) *(u32x4*)(dst + (size_t)(kq * 4 + k) * 3072) = fr;
    }
    if (tw < NT1 && (lane & 15) == 0) *(f32x4*)(p.ws + WS_CS + ((size_t)(kq * 180 + tw) * 16 + g4 * 4) * 4) = cs;
    return;
  }
  b -= 88;
  {
    const int s = b;
    const u32x4* src = (const u32x4*)(p.wd + (size_t)s * 32 * D_);
#pragma unroll
    for (int i = 0; i < 4; i++) {
      const int ch = i * 512 + tid;
      *(LAS u32x4*)(sm + (ch >> 6) * 1056 + (ch & 63) * 16) = src[ch];
    }
    __syncthreads();
    const int g4 = lane >> 4, q = (lane & 15) >> 2, pp = lane & 3;
    char* dst = p.ws + WS_W2P + ((size_t)(w * KS2 + s) * 4) * 1024 + lane * 16;
#pragma unroll
    for (int dt = 0; dt < 4; dt++) {
      LAS char* a = sm + (8 * g4 + q) * 1056 + (w * 4 + dt) * 32 + pp * 8;
      const u32x2 lo = tr_read(a), hi = tr_read(a + 4 * 1056);
      *(u32x4*)(dst + dt * 1024) = u32x4{lo.x, lo.y, hi.x, hi.y};
    }
  }
}

constexpr int CV_KC = 0;
constexpr int CV_U = 2 * 4 * 4160;
constexpr int CV_XS = CV_U + 2 * 4608;
constexpr int CV_RED = CV_XS + 16384;
constexpr int CV_LDS = CV_RED + 16384;
__global__ void __launch_bounds__(256) k_conv(const char* __restrict__ ws, const u16* __restrict__ xq, const double* __restrict__ stat,
                                              const u16* __restrict__ dskip, u16* __restrict__ xt) {
  extern __shared__ __attribute__((aligned(16))) char smem_[];
  LAS char* sm = (LAS char*)smem_;
  const int tid = threadIdx.x, lane = tid & 63, w = __builtin_amdgcn_readfirstlane(tid >> 6);
  const int b = blockIdx.x;
  const int cp = ((b >> 4) * 8 + (b & 7)) * 2 + ((b >> 3) & 1);
  const int par = cp & 1;
  f64x2 su[4], sq[4];
  u32x4 xv[4];
#pragma unroll
  for (int j = 0; j < 4; j++) {
    const int t = 2 * (tid + 256 * j);
    su[j] = *(const f64x2*)(stat + t);
    sq[j] = *(const f64x2*)(stat + 2048 + t);
    xv[j] = *(const u32x4*)(xq + ((size_t)(cp >> 1) * L_ + t) * 4);
  }
  u32x4 kv[2];
  u32x2 kn[2];
#pragma unroll
  for (int c = 0; c < 2; c++) {
    const char* kp = ws + WS_KR + (size_t)(cp * 2 + c) * 4096 + tid * 16;
    kv[c] = *(const u32x4*)kp;
    kn[c] = u32x2{0, 0};
    if (tid < 255) kn[c] = *(const u32x2*)(kp + 16);
  }
  const u32 dsk = *(const u32*)(dskip + cp * 2);
  const float kinv = ((const float*)(ws + WS_KSC))[cp * 2 + (w >> 1)];
  if (tid < 16) *(LAS u32x4*)(sm + CV_KC + (tid >> 1) * 4160 + 4096 + (tid & 1) * 16) = u32x4{0, 0, 0, 0};
  if (tid < 64) *(LAS u32x4*)(sm + CV_U + (tid >> 5) * 4608 + (tid & 31) * 16) = u32x4{0, 0, 0, 0};
#pragma unroll
  for (int c = 0; c < 2; c++) {
    const u32 w0 = kv[c].x, w1 = kv[c].y, w2 = kv[c].z, w3 = kv[c].w, w4 = kn[c].x, w5 = kn[c].y;
    const u32 a0 = __builtin_amdgcn_alignbit(w1, w0, 16), a1 = __builtin_amdgcn_alignbit(w2, w1, 16), a2 = __builtin_amdgcn_alignbit(w3, w2, 16),
              a3 = __builtin_amdgcn_alignbit(w4, w3, 16), a4 = __builtin_amdgcn_alignbit(w5, w4, 16);
    LAS char* dst = sm + CV_KC + c * (4 * 4160) + tid * 16;
    *(LAS u32x4*)(dst) = u32x4{w0, w1, w2, w3};
    *(LAS u32x4*)(dst + 4160) = u32x4{a0, a1, a2, a3};
    *(LAS u32x4*)(dst + 2 * 4160) = u32x4{w1, w2, w3, w4};
    *(LAS u32x4*)(dst + 3 * 4160) = u32x4{a1, a2, a3, a4};
  }
  const float ds0 = bf_lo(dsk), ds1 = bf_hi(dsk);
#pragma unroll
  for (int j = 0; j < 4; j++) {
    const int t = 2 * (tid + 256 * j);
    float m[2], ri[2];
#pragma unroll
    for (int i = 0; i < 2; i++) {
      const double mean = su[j][i] * (1.0 / D_);
      const double var = sq[j][i] * (1.0 / D_) - mean * mean;
      m[i] = rbf((float)mean);
      const float v = rbf(fmaxf((float)var, 0.f));
      const float sd = rbf(__builtin_amdgcn_sqrtf(rbf(v + 1e-5f)));
      ri[i] = 1.0f / sd;
    }
    const u32 xa = par ? xv[j].y : xv[j].x, xb = par ? xv[j].w : xv[j].z;
    const u32 d0 = pk_bf16(bf_lo(xa) - m[0], bf_lo(xb) - m[1]), d1 = pk_bf16(bf_hi(xa) - m[0], bf_hi(xb) - m[1]);
    const u32 u0 = pk_bf16(bf_lo(d0) * ri[0], bf_hi(d0) * ri[1]), u1 = pk_bf16(bf_lo(d1) * ri[0], bf_hi(d1) * ri[1]);
    const u32 s0 = pk_bf16(bf_lo(u0) * ds0, bf_hi(u0) * ds0), s1 = pk_bf16(bf_lo(u1) * ds1, bf_hi(u1) * ds1);
    *(LAS u32*)(sm + CV_U + 512 + t * 2) = pk_f16(bf_lo(u0), bf_hi(u0));
    *(LAS u32*)(sm + CV_U + 4608 + 512 + t * 2) = pk_f16(bf_lo(u1), bf_hi(u1));
    *(LAS u32x2*)(sm + CV_XS + t * 4) = u32x2{(xa & 0xffffu) | (s0 << 16), (xb & 0xffffu) | (s0 & 0xffff0000u)};
    *(LAS u32x2*)(sm + CV_XS + 8192 + t * 4) = u32x2{(xa >> 16) | (s1 << 16), (xb >> 16) | (s1 & 0xffff0000u)};
  }
  __syncthreads();
  const int c = w >> 1, half = w & 1, r = lane & 15, q = lane >> 4;
  const int cpy = (3 - r) & 3;
  LAS char* pa = sm + CV_KC + c * (4 * 4160) + cpy * 4160 + 2 * (15 + 8 * q - r - cpy) + 64 * (7 - 4 * half);
  LAS char* pb = sm + CV_U + c * 4608 + 2 * (240 + 16 * r + 8 * q) - 64 * (4 * half);
  f32x4 acc[8];
#pragma unroll
  for (int g = 0; g < 8; g++) acc[g] = f32x4{0.f, 0.f, 0.f, 0.f};
  u32x4 A[2][8], B[2][8];
#define CV_LOAD(buf, rr)                                                          \
  _Pragma("unroll") for (int i = 0; i < 8; i++) {                                 \
    B[buf][i] = *(LAS u32x4*)(pb + 512 * i - 64 * (rr));                          \
    const u32x2 lo = *(LAS u32x2*)(pa + 512 * (7 - i) - 64 * (rr));               \
    const u32x2 hi = *(LAS u32x2*)(pa + 512 * (7 - i) - 64 * (rr) + 8);           \
    A[buf][i] = u32x4{lo.x, lo.y, hi.x, hi.y};                                    \
  }
  CV_LOAD(0, 0)
#pragma unroll
  for (int rr = 0; rr < 4; rr++) {
    if (rr < 3) { CV_LOAD((rr + 1) & 1, rr + 1) }
#pragma unroll
    for (int s = 0; s < 8; s++)
#pragma unroll
      for (int i = 0; i <= s; i++) {
        if (i + s < 8) acc[i + s] = mfma_f16(A[rr & 1][i], B[rr & 1][s], acc[i + s]);
        if (i < s && i + s < 8) acc[i + s] = mfma_f16(A[rr & 1][s], B[rr & 1][i], acc[i + s]);
      }
  }
#undef CV_LOAD
  f32x4 keep[4];
  {
    LAS char* red = sm + CV_RED + w * 4096 + lane * 16;
    if (half) {
#pragma unroll
      for (int g = 0; g < 4; g++) { *(LAS f32x4*)(red + g * 1024) = acc[g]; keep[g] = acc[4 + g]; }
    } else {
#pragma unroll
      for (int g = 0; g < 4; g++) { *(LAS f32x4*)(red + g * 1024) = acc[4 + g]; keep[g] = acc[g]; }
    }
  }
  __syncthreads();
  {
    LAS char* red = sm + CV_RED + (w ^ 1) * 4096 + lane * 16;
    u16* out = xt + (size_t)(cp * 2 + c) * L_;
#pragma unroll
    for (int g = 0; g < 4; g++) {
      const f32x4 v = (keep[g] + *(LAS f32x4*)(red + g * 1024)) * kinv;
      const int t0 = 256 * (4 * half + g) + 16 * r + 4 * q;
      const u32x4 xs = *(LAS u32x4*)(sm + CV_XS + c * 8192 + t0 * 4);
      const u32 y0 = pk_bf16(v.x, v.y), y1 = pk_bf16(v.z, v.w);
      const u32 z0 = pk_bf16(bf_lo(y0) + bf_hi(xs.x), bf_hi(y0) + bf_hi(xs.y)), z1 = pk_bf16(bf_lo(y1) + bf_hi(xs.z), bf_hi(y1) + bf_hi(xs.w));
      const u32 o0 = pk_bf16(bf_lo(z0) + bf_lo(xs.x), bf_hi(z0) + bf_lo(xs.y)), o1 = pk_bf16(bf_lo(z1) + bf_lo(xs.z), bf_hi(z1) + bf_lo(xs.w));
      *(u32x2*)(out + t0) = u32x2{o0, o1};
    }
  }
}

constexpr int G1_LDS = 131072;
#define ASM_LD16(dst, voff, sbase, imm) asm volatile("global_load_dwordx4 %0, %1, %2 offset:" #imm : "=v"(dst) : "v"(voff), "s"(sbase) : "memory")
template <int N> DEVI void wait_w(u32x4 (&f)[3]) {
  asm volatile("s_waitcnt vmcnt(%3)" : "+v"(f[0]), "+v"(f[1]), "+v"(f[2]) : "n"(N) : "memory");
}
template <int N> DEVI void wait_v() { asm volatile("s_waitcnt vmcnt(%0)" ::"n"(N) : "memory"); }

__global__ void __launch_bounds__(256, 1) k_gemm1(char* __restrict__ ws, const u16* __restrict__ xt, u64* __restrict__ gran) {
  extern __shared__ __attribute__((aligned(16))) char smem_[];
  LAS char* sm = (LAS char*)smem_;
  const int tid = threadIdx.x, lane = tid & 63, w = __builtin_amdgcn_readfirstlane(tid >> 6);
  const int b = blockIdx.x, xcd = b & 7, idx = b >> 3;
  const int tt = (xcd >> 1) * 4 + (idx & 3), chunk = (xcd & 1) * 8 + (idx >> 2);
  const int t0 = tt * 128;
  const int g4 = lane >> 4, q = (lane & 15) >> 2, pp = lane & 3;
  const u32 xv0 = ((8 * w + g4) * L_ + t0 + 16 * (((lane & 15) >> 1) ^ (g4 | ((w & 1) << 2))) + 8 * (lane & 1)) * 2;
  const u32 xv1 = xv0 + 4 * L_ * 2 - 1024;
  const u32 ldsw = (u32)(uintptr_t)sm + w * 2048;
#define G1_STAGE(k)                                                                                                        \
  asm volatile("s_mov_b32 m0, %3\n\ts_nop 0\n\tglobal_load_lds_dwordx4 %0, %2\n\tglobal_load_lds_dwordx4 %1, %2 offset:1024" \
               ::"v"(xv0), "v"(xv1), "s"(xt + (size_t)(32 * (k)) * L_), "s"(ldsw + (k) * 8192) : "memory");
  LAS char* xr = sm + (8 * g4 + q) * 256 + pp * 8;
  const int fsw = q | ((g4 & 1) << 2);
  if (chunk == 15) {
#pragma unroll
    for (int k = 0; k < 16; k++) { G1_STAGE(k) }
    const u32x4 ones = {0x3f803f80u, 0x3f803f80u, 0x3f803f80u, 0x3f803f80u};
    f32x4 as[2], aq[2];
#pragma unroll
    for (int i = 0; i < 2; i++) { as[i] = f32x4{0.f, 0.f, 0.f, 0.f}; aq[i] = as[i]; }
#pragma unroll
    for (int kk = 0; kk < 4; kk++) {
      if (kk == 0) wait_v<24>(); else if (kk == 1) wait_v<16>(); else if (kk == 2) wait_v<8>(); else wait_v<0>();
      __builtin_amdgcn_s_barrier();
#pragma unroll
      for (int k = 4 * kk; k < 4 * kk + 4; k++)
#pragma unroll
        for (int i = 0; i < 2; i++) {
          LAS char* a = xr + k * 8192 + 32 * ((2 * w + i) ^ fsw);
          const u32x2 lo = tr_read(a), hi = tr_read(a + 1024);
          const u32x4 fr = {lo.x, lo.y, hi.x, hi.y};
          as[i] = mfma_bf16(ones, fr, as[i]);
          aq[i] = mfma_bf16(fr, fr, aq[i]);
        }
    }
    if (q == g4) {
#pragma unroll
      for (int i = 0; i < 2; i++) {
        const float ssq = pp == 0 ? aq[i][0] : pp == 1 ? aq[i][1] : pp == 2 ? aq[i][2] : aq[i][3];
        const float mean = as[i][0] * (1.f / D_);
        const float var = fmaxf(ssq * (1.f / D_) - mean * mean, 0.f);
        const float sd = __builtin_amdgcn_sqrtf(rbf(rbf(var) + 1e-5f));
        const u64 gv = ((u64)GTAG << 32) | pk_bf16(mean, sd);
        __hip_atomic_store((GAS u64*)(gran + t0 + (2 * w + i) * 16 + (lane & 15)), gv, RLX_AGENT);
      }
    }
    return;
  }
  const int grp = chunk * 4 + w;
  const char* wb = ws + WS_W1P + (size_t)grp * 49152;
  const u32 voff = lane * 16;
  u32x4 wf[16][3];
#define G1_ISSUE(k)                        \
  {                                        \
    G1_STAGE(k)                            \
    const char* bk = wb + (k) * 3072;      \
    ASM_LD16(wf[k][0], voff, bk, 0);       \
    ASM_LD16(wf[k][1], voff, bk, 1024);    \
    ASM_LD16(wf[k][2], voff, bk, 2048);    \
  }
#pragma unroll
  for (int k = 0; k < 12; k++) G1_ISSUE(k)
  f32x4 acc[8][3];
#pragma unroll
  for (int i = 0; i < 8; i++)
#pragma unroll
    for (int j = 0; j < 3; j++) acc[i][j] = f32x4{0.f, 0.f, 0.f, 0.f};
  u32x4 xf[2][8];
#define G1_READ(k)                                             \
  _Pragma("unroll") for (int i = 0; i < 8; i++) {              \
    LAS char* a = xr + (k) * 8192 + 32 * (i ^ fsw);            \
    const u32x2 lo = tr_read(a), hi = tr_read(a + 1024);       \
    xf[(k) & 1][i] = u32x4{lo.x, lo.y, hi.x, hi.y};            \
  }
  u32x2 gr[8];
  wait_w<55>(wf[0]);
  __builtin_amdgcn_s_barrier();
  G1_READ(0)
#define G1_STEP(k, NW)                                                                          \
  {                                                                                             \
    if ((k) + 12 < 16) G1_ISSUE(((k) + 12) & 15)                                                \
    if ((k) == 11) {                                                                            \
      _Pragma("unroll") for (int i = 0; i < 8; i++)                                             \
        asm volatile("global_load_dwordx2 %0, %1, %2 offset:%3 sc1" : "=v"(gr[i]) : "v"((lane & 15) * 8), "s"(gran + t0), "n"(i * 128) : "memory"); \
    }                                                                                           \
    if ((k) < 15) {                                                                             \
      wait_w<NW>(wf[((k) + 1) & 15]);                                                           \
      __builtin_amdgcn_s_barrier();                                                             \
      G1_READ(((k) + 1) & 15)                                                                   \
    }                                                                                           \
    _Pragma("unroll") for (int i = 0; i < 8; i++)                                               \
      _Pragma("unroll") for (int j = 0; j < 3; j++) acc[i][j] = mfma_bf16(wf[k][j], xf[(k) & 1][i], acc[i][j]); \
  }
  G1_STEP(0, 55) G1_STEP(1, 55) G1_STEP(2, 55) G1_STEP(3, 55)
  G1_STEP(4, 50) G1_STEP(5, 45) G1_STEP(6, 40) G1_STEP(7, 35)
  G1_STEP(8, 30) G1_STEP(9, 25) G1_STEP(10, 20) G1_STEP(11, 23)
  G1_STEP(12, 18) G1_STEP(13, 13) G1_STEP(14, 8) G1_STEP(15, 0)
  f32x4 cs[3];
#pragma unroll
  for (int j = 0; j < 3; j++) {
    const float* cp = (const float*)(ws + WS_CS) + (grp * 3 + j) * 16 + g4 * 4;
    cs[j] = (*(const f32x4*)cp + *(const f32x4*)(cp + 180 * 16)) + (*(const f32x4*)(cp + 2 * 180 * 16) + *(const f32x4*)(cp + 3 * 180 * 16));
  }
  asm volatile("s_waitcnt vmcnt(0)" : "+v"(gr[0]), "+v"(gr[1]), "+v"(gr[2]), "+v"(gr[3]), "+v"(gr[4]), "+v"(gr[5]), "+v"(gr[6]), "+v"(gr[7])::"memory");
  {
    bool ok = true;
#pragma unroll
    for (int i = 0; i < 8; i++) ok &= gr[i].y == GTAG;
    for (int spin = 0; !__all(ok) && spin < (1 << 20); spin++) {
      ok = true;
#pragma unroll
      for (int i = 0; i < 8; i++) {
        const u64 v = __hip_atomic_load((GAS u64*)(gran + t0 + i * 16 + (lane & 15)), RLX_AGENT);
        gr[i] = u32x2{(u32)v, (u32)(v >> 32)};
        ok &= gr[i].y == GTAG;
      }
    }
  }
  u32 hv[24];
#pragma unroll
  for (int i = 0; i < 8; i++) {
    const float m = bf_lo(gr[i].x), ri = __builtin_amdgcn_rcpf(bf_hi(gr[i].x));
    const float kx = -1.44269504088896341f * ri, r2 = ri * ri;
#pragma unroll
    for (int j = 0; j < 3; j++) {
      const f32x4 a = acc[i][j] - m * cs[j];
      const float e0 = __builtin_amdgcn_exp2f(a.x * kx), e1 = __builtin_amdgcn_exp2f(a.y * kx);
      const float h0 = a.x * a.z * r2 * __builtin_amdgcn_rcpf(1.f + e0), h1 = a.y * a.w * r2 * __builtin_amdgcn_rcpf(1.f + e1);
      hv[i * 3 + j] = pk_bf16(h0, h1);
    }
  }
  char* hb = ws + WS_H + (size_t)(tt * 8) * (NT1 * 256) + (size_t)(grp * 3) * 256 + (lane & 15) * 16;
#pragma unroll
  for (int n = 0; n < 6; n++) {
    u32 r0 = hv[4 * n], r1 = hv[4 * n + 1], r2 = hv[4 * n + 2], r3 = hv[4 * n + 3];
    auto s02 = __builtin_amdgcn_permlane32_swap(r0, r2, false, false);
    auto s13 = __builtin_amdgcn_permlane32_swap(r1, r3, false, false);
    auto a01 = __builtin_amdgcn_permlane16_swap(s02[0], s13[0], false, false);
    auto a23 = __builtin_amdgcn_permlane16_swap(s02[1], s13[1], false, false);
    const int item = 4 * n + g4, ii = item / 3, jj = item - 3 * ii;
    if (grp * 3 + jj < NT1) *(u32x4*)(hb + (size_t)ii * (NT1 * 256) + jj * 256) = u32x4{a01[0], a01[1], a23[0], a23[1]};
  }
}

constexpr int G2_RES = 65536;
constexpr int G2_LDS = G2_RES + 64 * 160;
template <bool LAST>
__global__ void __launch_bounds__(256, 1) k_gemm2(const char* __restrict__ ws, const u16* __restrict__ xt, u16* __restrict__ out, double* __restrict__ stat) {
  extern __shared__ __attribute__((aligned(16))) char smem_[];
  LAS char* sm = (LAS char*)smem_;
  const int tid = threadIdx.x, lane = tid & 63, w = __builtin_amdgcn_readfirstlane(tid >> 6);
  const int b = blockIdx.x, xcd = b & 7, idx = b >> 3;
  const int tb = (xcd >> 1) * 8 + (idx & 7), db = (xcd & 1) * 4 + (idx >> 3);
  const int r = lane & 15, g4 = lane >> 4;
  const int s0 = 11 * w - 1;
  const char* ap = ws + WS_W2P + ((size_t)(db * KS2 + s0) * 4) * 1024 + lane * 16;
  const char* bp = ws + WS_H + (size_t)(tb * 4) * (NT1 * 256) + (size_t)s0 * 1024 + lane * 16;
  constexpr int RING = 6;
  u32x4 A[11][4], B[11][4];
#define G2_LOAD(s)                                                                \
  _Pragma("unroll") for (int i = 0; i < 4; i++) {                                 \
    B[s][i] = *(const u32x4*)(bp + (size_t)i * (NT1 * 256) + (s) * 1024);         \
    A[s][i] = *(const u32x4*)(ap + (s) * 4096 + i * 1024);                        \
  }
  f32x4 acc[4][4];
#pragma unroll
  for (int i = 0; i < 4; i++)
#pragma unroll
    for (int j = 0; j < 4; j++) acc[i][j] = f32x4{0.f, 0.f, 0.f, 0.f};
  if (w > 0) { G2_LOAD(0) }
  else {
#pragma unroll
    for (int i = 0; i < 4; i++) { A[0][i] = u32x4{0, 0, 0, 0}; B[0][i] = u32x4{0, 0, 0, 0}; }
  }
#pragma unroll
  for (int s = 1; s < RING; s++) { G2_LOAD(s) }
  u32x4 rs[2];
#pragma unroll
  for (int i = 0; i < 2; i++) {
    const int ch = tid + 256 * i;
    rs[i] = *(const u32x4*)(xt + (size_t)(db * 64 + (ch >> 3)) * L_ + tb * 64 + (ch & 7) * 8);
  }
  __builtin_amdgcn_sched_barrier(0);
#pragma unroll
  for (int s = 0; s < 11; s++) {
#pragma unroll
    for (int i = 0; i < 4; i++)
#pragma unroll
      for (int j = 0; j < 4; j++) acc[i][j] = mfma_bf16(A[s][j], B[s][i], acc[i][j]);
    if (s + RING < 11) { G2_LOAD(s + RING) }
    __builtin_amdgcn_sched_barrier(0);
  }
#undef G2_LOAD
#pragma unroll
  for (int i = 0; i < 2; i++) {
    const int ch = tid + 256 * i;
    *(LAS u32x4*)(sm + G2_RES + (ch >> 3) * 160 + (ch & 7) * 16) = rs[i];
  }
  f32x4 own[4];
#pragma unroll
  for (int i = 0; i < 4; i++) {
    if (i == w) {
#pragma unroll
      for (int j = 0; j < 4; j++) own[j] = acc[i][j];
    } else {
#pragma unroll
      for (int j = 0; j < 4; j++) *(LAS f32x4*)(sm + ((w * 4 + i) * 4 + j) * 1024 + lane * 16) = acc[i][j];
    }
  }
  __syncthreads();
#pragma unroll
  for (int o = 1; o < 4; o++) {
    const int sw = (w + o) & 3;
#pragma unroll
    for (int j = 0; j < 4; j++) own[j] += *(LAS f32x4*)(sm + ((sw * 4 + w) * 4 + j) * 1024 + lane * 16);
  }
  const int t = tb * 64 + w * 16 + r;
  float s1 = 0.f, s2 = 0.f;
#pragma unroll
  for (int j = 0; j < 4; j++) {
    const u32x2 xr = tr_read(sm + G2_RES + (16 * j + 4 * g4 + (r >> 2)) * 160 + w * 32 + (lane & 3) * 8);
    const u32 y0 = pk_bf16(own[j].x, own[j].y), y1 = pk_bf16(own[j].z, own[j].w);
    const u32 o0 = pk_bf16(bf_lo(xr.x) + bf_lo(y0), bf_hi(xr.x) + bf_hi(y0)), o1 = pk_bf16(bf_lo(xr.y) + bf_lo(y1), bf_hi(xr.y) + bf_hi(y1));
    const int d = db * 64 + 16 * j + 4 * g4;
    if (LAST) {
      *(u32x2*)(out + (size_t)t * D_ + d) = u32x2{o0, o1};
    } else {
      *(u32x2*)(out + ((size_t)(d >> 2) * L_ + t) * 4) = u32x2{o0, o1};
      const float a = bf_lo(o0), bq = bf_hi(o0), cq = bf_lo(o1), dq = bf_hi(o1);
      s1 += (a + bq) + (cq + dq);
      s2 += (a * a + bq * bq) + (cq * cq + dq * dq);
    }
  }
  if (!LAST) {
    s1 += __shfl_xor(s1, 16); s2 += __shfl_xor(s2, 16);
    s1 += __shfl_xor(s1, 32); s2 += __shfl_xor(s2, 32);
    if (lane < 32) __hip_atomic_fetch_add(stat + g4 * 2048 + t, (double)(g4 ? s2 : s1), RLX_AGENT);
  }
}

extern "C" void kernel_launch(void* const* d_in, const int* in_sizes, int n_in, void* d_out, int out_size, void* d_ws, size_t ws_size,
                              hipStream_t stream) {
  char* ws = (char*)d_ws;
  (void)hipFuncSetAttribute((const void*)k_prep, hipFuncAttributeMaxDynamicSharedMemorySize, K0_LDS);
  (void)hipFuncSetAttribute((const void*)k_conv, hipFuncAttributeMaxDynamicSharedMemorySize, CV_LDS);
  (void)hipFuncSetAttribute((const void*)k_gemm1, hipFuncAttributeMaxDynamicSharedMemorySize, G1_LDS);
  (void)hipFuncSetAttribute((const void*)k_gemm2<false>, hipFuncAttributeMaxDynamicSharedMemorySize, G2_LDS);
  (void)hipFuncSetAttribute((const void*)k_gemm2<true>, hipFuncAttributeMaxDynamicSharedMemorySize, G2_LDS);
  P0 p;
  p.x0 = (const u16*)d_in[0]; p.are = (const u16*)d_in[1]; p.aim = (const u16*)d_in[2]; p.bre = (const u16*)d_in[3]; p.bim = (const u16*)d_in[4];
  p.cre = (const u16*)d_in[5]; p.cim = (const u16*)d_in[6]; p.logdt = (const u16*)d_in[7];
  p.wg = (const u16*)d_in[10]; p.wu = (const u16*)d_in[11]; p.wd = (const u16*)d_in[12]; p.ws = ws;
  const u16* dskip = (const u16*)d_in[8];
  k_prep<<<256 + 128 + 88 + KS2, 512, K0_LDS, stream>>>(p);
  double* stat = (double*)(ws + WS_STAT);
  u64* gran = (u64*)(ws + WS_GRAN);
  k_conv<<<256, 256, CV_LDS, stream>>>(ws, (const u16*)(ws + WS_XQ0), stat, dskip, (u16*)(ws + WS_XT1));
  k_gemm1<<<256, 256, G1_LDS, stream>>>(ws, (const u16*)(ws + WS_XT1), gran);
  k_gemm2<false><<<256, 256, G2_LDS, stream>>>(ws, (const u16*)(ws + WS_XT1), (u16*)(ws + WS_XQ2), stat + 4096);
  k_conv<<<256, 256, CV_LDS, stream>>>(ws, (const u16*)(ws + WS_XQ2), stat + 4096, dskip, (u16*)(ws + WS_XT3));
  k_gemm1<<<256, 256, G1_LDS, stream>>>(ws, (const u16*)(ws + WS_XT3), gran + 2048);
  k_gemm2<true><<<256, 256, G2_LDS, stream>>>(ws, (const u16*)(ws + WS_XT3), (u16*)d_out, stat);
}
```

```cpp
#include <hip/hip_runtime.h>
#include <stdint.h>

typedef unsigned short u16;
typedef unsigned int u32;
typedef unsigned long long u64;
typedef float f32x2 __attribute__((ext_vector_type(2)));
typedef float f32x4 __attribute__((ext_vector_type(4)));
typedef double f64x2 __attribute__((ext_vector_type(2)));
typedef u32 u32x2 __attribute__((ext_vector_type(2)));
typedef u32 u32x4 __attribute__((ext_vector_type(4)));
typedef short s16x4 __attribute__((ext_vector_type(4)));
typedef __bf16 bf16x2 __attribute__((ext_vector_type(2)));
typedef __bf16 bf16x8 __attribute__((ext_vector_type(8)));
typedef _Float16 f16x2 __attribute__((ext_vector_type(2)));
typedef _Float16 f16x8 __attribute__((ext_vector_type(8)));
#define LAS __attribute__((address_space(3)))
#define GAS __attribute__((address_space(1)))
#define DEVI __device__ __forceinline__
#define RLX_AGENT __ATOMIC_RELAXED, __HIP_MEMORY_SCOPE_AGENT

constexpr int L_ = 2048, D_ = 512, N_ = 128, F_ = 1376;
constexpr int NT1 = 172;
constexpr int KS2 = 43;

constexpr size_t WS_KR = 0;
constexpr size_t WS_KSC = WS_KR + (size_t)D_ * L_ * 2;
constexpr size_t WS_STAT = WS_KSC + 4096;
constexpr size_t WS_GRAN = WS_STAT + 2 * 32768;
constexpr size_t WS_CS = WS_GRAN + 2 * 16384;
constexpr size_t WS_XQ0 = WS_CS + 65536;
constexpr size_t WS_XQ2 = WS_XQ0 + (2u << 20);
constexpr size_t WS_XT1 = WS_XQ2 + (2u << 20);
constexpr size_t WS_XT3 = WS_XT1 + (2u << 20);
constexpr size_t WS_W1P = WS_XT3 + (2u << 20);
constexpr size_t WS_W2P = WS_W1P + (size_t)64 * 16 * 3 * 1024;
constexpr size_t WS_H = WS_W2P + (size_t)8 * KS2 * 4 * 1024;
constexpr size_t H_BYTES = (size_t)128 * NT1 * 256;
constexpr size_t WS_CNT = WS_H + 2 * H_BYTES;
constexpr int CNT_LOC = 6 * 256;
constexpr int CNT_LINES = CNT_LOC + 64;
constexpr size_t WS_END = WS_CNT + CNT_LINES * 128;
constexpr size_t WS_CSP = 0, WS_EPOCH = 1u << 20, WS_TAGS = WS_EPOCH + 65536;
#define STAMP_DECL(kid, wsp)
#define STAMP(i) {}
#define STAMP_END() {}
#define DS(i) {}
#define LDS_SZ(x) (x)
#define STAMP_ARG
#define STAMP_PASS

DEVI float bf_lo(u32 v) { return __uint_as_float(v << 16); }
DEVI float bf_hi(u32 v) { return __uint_as_float(v & 0xffff0000u); }
DEVI u32 pk_bf16(float a, float b) { f32x2 v = {a, b}; return __builtin_bit_cast(u32, __builtin_convertvector(v, bf16x2)); }
DEVI u32 pk_f16(float a, float b) { f32x2 v = {a, b}; return __builtin_bit_cast(u32, __builtin_convertvector(v, f16x2)); }
DEVI float rbf(float a) { return bf_lo(pk_bf16(a, a)); }
DEVI f32x4 mfma_bf16(u32x4 a, u32x4 b, f32x4 c) {
  return __builtin_amdgcn_mfma_f32_16x16x32_bf16(__builtin_bit_cast(bf16x8, a), __builtin_bit_cast(bf16x8, b), c, 0, 0, 0);
}
DEVI f32x4 mfma_f16(u32x4 a, u32x4 b, f32x4 c) {
  return __builtin_amdgcn_mfma_f32_16x16x32_f16(__builtin_bit_cast(f16x8, a), __builtin_bit_cast(f16x8, b), c, 0, 0, 0);
}
DEVI u32x2 tr_read(LAS char* p) { return __builtin_bit_cast(u32x2, __builtin_amdgcn_ds_read_tr16_b64_v4i16((LAS s16x4*)p)); }
DEVI float vmax(float a, float b) { float d; asm("v_max_f32 %0, %1, %2" : "=v"(d) : "v"(a), "v"(b)); return d; }
DEVI void wave_max2(float& x, float& y) {
  auto a = __builtin_amdgcn_permlane32_swap(__float_as_uint(x), __float_as_uint(y), false, false);
  float v = vmax(__uint_as_float(a[0]), __uint_as_float(a[1]));
#define DPP_MAX(ctrl) v = vmax(v, __builtin_bit_cast(float, __builtin_amdgcn_update_dpp(0, __builtin_bit_cast(int, v), ctrl, 0xf, 0xf, true)))
  DPP_MAX(0x128); DPP_MAX(0x124); DPP_MAX(0x122); DPP_MAX(0x121);
#undef DPP_MAX
  x = vmax(__builtin_bit_cast(float, __builtin_amdgcn_readlane(__builtin_bit_cast(int, v), 0)), __builtin_bit_cast(float, __builtin_amdgcn_readlane(__builtin_bit_cast(int, v), 16)));
  y = vmax(__builtin_bit_cast(float, __builtin_amdgcn_readlane(__builtin_bit_cast(int, v), 32)), __builtin_bit_cast(float, __builtin_amdgcn_readlane(__builtin_bit_cast(int, v), 48)));
}
DEVI f32x2 unp(u32 v) { return f32x2{bf_lo(v), bf_hi(v)}; }
DEVI u32 pkb(f32x2 v) { return __builtin_bit_cast(u32, __builtin_convertvector(v, bf16x2)); }
DEVI u32 pkh(f32x2 v) { return __builtin_bit_cast(u32, __builtin_convertvector(v, f16x2)); }
struct cx2 { f32x2 re, im; };
DEVI cx2 cmul(cx2 a, cx2 b) { return cx2{a.re * b.re - a.im * b.im, a.re * b.im + a.im * b.re}; }
DEVI cx2 cexp2(f32x2 zl, f32x2 zt, float m, float lg) {
  f32x2 t = zt * m, e = zl * m + lg;
  t -= f32x2{__builtin_rintf(t.x), __builtin_rintf(t.y)};
  e = f32x2{__builtin_amdgcn_exp2f(e.x), __builtin_amdgcn_exp2f(e.y)};
  return cx2{e * f32x2{__builtin_amdgcn_cosf(t.x), __builtin_amdgcn_cosf(t.y)}, e * f32x2{__builtin_amdgcn_sinf(t.x), __builtin_amdgcn_sinf(t.y)}};
}

struct P0 {
  const u16 *x0, *are, *aim, *bre, *bim, *cre, *cim, *logdt, *wg, *wu, *wd, *dskip;
  char* ws;
  u16* out;
};
#define P_GRL 13
#define P_CHS 14
#define P_CHP 2
#define P_CHC 46
#define P_ALP 4
#define P_BLP 10
#define P_BHS 44
#define P_BHP 85
#define P_CLP 34
#define G2MAP 0
#define HPRE 10
#define TOUCH 0
#define TSTRIDE (1 << 21)
#define T2_EARLY 1
constexpr int AUX_SC1 = 16;
typedef __amdgpu_buffer_rsrc_t rsrc_t;
DEVI u32x4 ld16(rsrc_t rs, u32 voff, u32 soff) { return __builtin_amdgcn_raw_buffer_load_b128(rs, voff, soff, AUX_SC1); }
DEVI void st16(rsrc_t rs, u32 voff, u32 soff, u32x4 v) { __builtin_amdgcn_raw_buffer_store_b128(v, rs, voff + soff, 0, AUX_SC1); }
DEVI void st8(rsrc_t rs, u32 voff, u32 soff, u32x2 v) { __builtin_amdgcn_raw_buffer_store_b64(v, rs, voff, soff, AUX_SC1); }
#define SB() __builtin_amdgcn_sched_barrier(0)
#define BAR() { SB(); asm volatile("s_waitcnt lgkmcnt(0)" ::: "memory"); __builtin_amdgcn_s_barrier(); asm volatile("" ::: "memory"); SB(); }
#define BARX() { SB(); asm volatile("" ::: "memory"); __builtin_amdgcn_s_barrier(); asm volatile("" ::: "memory"); SB(); }
template <int N> DEVI void wait_v() { asm volatile("s_waitcnt vmcnt(%0)" ::"n"(N) : "memory"); }
constexpr int SPIN_CAP = 1 << 18;
DEVI u32 opq(u32 x) { asm volatile("" : "+s"(x)); return x; }
template <int V> struct IC { static constexpr int v = V; constexpr operator int() const { return V; } };
template <int I, int N, class F> DEVI void sfor_(F&& f) { if constexpr (I < N) { f(IC<I>{}); sfor_<I + 1, N>(f); } }
template <int N, class F> DEVI void sfor(F&& f) { sfor_<0, N>(f); }

__device__ double buf_fadd_f64(double, __amdgpu_buffer_rsrc_t, int, int, int) __asm("llvm.amdgcn.raw.ptr.buffer.atomic.fadd.f64");
__device__ long buf_add_i64(long, __amdgpu_buffer_rsrc_t, int, int, int) __asm("llvm.amdgcn.raw.ptr.buffer.atomic.add.i64");
template <class T> DEVI void pin(T& x) { asm volatile("" : "+v"(x)); }
constexpr u32 OOB = 0x7f000000u;
template <bool Z> DEVI void mmA(f32x4& d, const u32x4& a, const u32x4& b) {
  if constexpr (Z) asm volatile("v_mfma_f32_16x16x32_bf16 %0, %1, %2, 0" : "=&v"(d) : "a"(a), "v"(b));
  else asm volatile("v_mfma_f32_16x16x32_bf16 %0, %1, %2, %0" : "+v"(d) : "a"(a), "v"(b));
}
DEVI void mmV(f32x4& d, const u32x4& a, const u32x4& b) { asm volatile("v_mfma_f32_16x16x32_bf16 %0, %1, %2, %0" : "+v"(d) : "v"(a), "v"(b)); }
template <bool Z> DEVI void mmH(f32x4& d, const u32x4& a, const u32x4& b) {
  if constexpr (Z) asm volatile("v_mfma_f32_16x16x32_f16 %0, %1, %2, 0" : "=&v"(d) : "v"(a), "v"(b));
  else asm volatile("v_mfma_f32_16x16x32_f16 %0, %1, %2, %0" : "+v"(d) : "v"(a), "v"(b));
}
#define DPP_ADD(v, ctrl) v += __builtin_bit_cast(float, __builtin_amdgcn_update_dpp(0, __builtin_bit_cast(int, v), ctrl, 0xf, 0xf, true))
#define MM_DONE() { asm volatile("s_nop 7" ::: "memory"); SB(); }

DEVI void sig_all(rsrc_t rs, int ev, int b, int tid, bool en) {
  __builtin_amdgcn_raw_ptr_buffer_atomic_add_i32(1, rs, tid < (en ? 16 : 0) ? tid * 2048 : OOB, (u32)WS_CNT + (ev * 256 + (b & 15)) * 128, 0);
  SB();
}
DEVI void sig_loc(rsrc_t rs, int ev, int tid, bool en) {
  __builtin_amdgcn_raw_ptr_buffer_atomic_add_i32(1, rs, tid < (en ? 1 : 0) ? 0 : OOB, (u32)WS_CNT + (CNT_LOC + ev) * 128, 0);
  SB();
}
DEVI u32 po_all(int ev, int b) { return (u32)WS_CNT + (ev * 256 + (b & 15) * 16) * 128; }
DEVI u32 po_loc(int ev) { return (u32)WS_CNT + (CNT_LOC + ev) * 128; }
DEVI u32 poll_issue(rsrc_t rs, u32 vo, u32 so) {
  asm volatile("" ::: "memory");
  return (u32)__builtin_amdgcn_raw_ptr_buffer_atomic_add_i32(0, rs, vo, so, 0);
}
#define SL_BEGIN()
#define SL_END(id)
#define SL_ARG
#define SL(id)
DEVI void poll_check(rsrc_t rs, u32 vo, u32 so, u32 v, u32 need SL_ARG) {
  pin(v);
  if (__builtin_expect(!__all(v >= need), 0)) {
    SL_BEGIN()
#pragma unroll 1
    for (int spin = 0; spin < SPIN_CAP; spin++) {
      if (__all(poll_issue(rs, vo, so) >= need)) break;
      __builtin_amdgcn_s_sleep(2);
    }
    SL_END(slid)
  }
  asm volatile("" ::: "memory");
}

DEVI u32x2 tc_add(rsrc_t rs, u32 vo, u32 so, long v) {
  asm volatile("" ::: "memory");
  return __builtin_bit_cast(u32x2, buf_add_i64(v, rs, vo, so, 0));
}
DEVI u32x2 tc_arrive(rsrc_t rs, int ev, int b, int tid) { return tc_add(rs, tid < 16 ? tid * 2048 : OOB, (u32)WS_TAGS + (ev * 256 + (b & 15)) * 128, 1); }
DEVI void tc_arrive_check(rsrc_t rs, int ev, int b, int tid, u32x2 v, u32 tag) {
  pin(v);
  bool todo = tid < 16 && v.y != tag;
  if (__builtin_expect(__any(todo), 0)) {
#pragma unroll 1
    for (int spin = 0; spin < SPIN_CAP; spin++) {
      __builtin_amdgcn_s_sleep(2);
      v = tc_add(rs, todo ? tid * 2048 : OOB, (u32)WS_TAGS + (ev * 256 + (b & 15)) * 128, 1);
      todo = todo && v.y != tag;
      if (!__any(todo)) break;
    }
  }
}
DEVI u32x2 tc_poll(rsrc_t rs, int ev, int b, int tid) { return tc_add(rs, tid < 16 ? tid * 128 : OOB, (u32)WS_TAGS + (ev * 256 + (b & 15) * 16) * 128, 0); }
DEVI void tc_poll_check(rsrc_t rs, int ev, int b, int tid, u32x2 v, u32 tag) {
  pin(v);
  if (__builtin_expect(__any(tid < 16 && (v.y != tag || v.x < 16u)), 0)) {
#pragma unroll 1
    for (int spin = 0; spin < SPIN_CAP; spin++) {
      v = tc_poll(rs, ev, b, tid);
      if (!__any(tid < 16 && (v.y != tag || v.x < 16u))) break;
      __builtin_amdgcn_s_sleep(2);
    }
  }
  asm volatile("" ::: "memory");
}

constexpr int KCS = 4128;
constexpr int M_KC = 0;
constexpr int M_W = 8 * KCS;
constexpr int M_U = M_W;
constexpr int M_XS = M_U + 2 * 4608;
constexpr int M_CRED = M_XS + 8192;
constexpr int M_X = M_W + 15 * 4096;
constexpr int M_H = M_W;
constexpr int M_RES = M_H + 22 * 4096;
constexpr int M_LDS = M_W + 31 * 4096;

template <int LOFF, int GOFF> DEVI void glds(u32 voff, rsrc_t rs, u32 lbase, u32 gbase) {
  u32 tmp;
  asm volatile("s_add_u32 m0, %3, %5\n\ts_add_u32 %0, %4, %6\n\tbuffer_load_dwordx4 %1, %2, %0 offen sc1 lds"
               : "=&s"(tmp) : "v"(voff), "s"(rs), "s"(lbase), "s"(gbase), "n"(LOFF), "n"(GOFF) : "memory", "scc");
}

template <bool STATS>
DEVI void main_body(LAS char* sm, char* __restrict__ ws, u16* __restrict__ out, const u32 GTAG, const float kinv,
                    u32x4 (&in_su)[2], u32x4 (&in_sq)[2], u32x4 (&in_xv)[2], u32x4 (&wf)[48], f32x4 (&cs)[3] STAMP_ARG) {
  const int w = __builtin_amdgcn_readfirstlane(threadIdx.x >> 6);
#define TIDS() int tid = threadIdx.x; asm volatile("" : "+v"(tid)); const int lane = tid & 63, r = lane & 15, g4 = lane >> 4; (void)r; (void)g4;
  const int b = blockIdx.x, xcd = b & 7, idx = b >> 3;
  const int cp = ((b >> 4) * 8 + (b & 7)) * 2 + ((b >> 3) & 1);
  const int tt = (xcd >> 1) * 4 + (idx & 3), chunk = (xcd & 1) * 8 + (idx >> 2);
  const int tb = (xcd >> 1) * 8 + (idx & 7), db = (xcd & 1) * 4 + (idx >> 3);
  const int grp = chunk * 4 + w;
  const rsrc_t rs = __builtin_amdgcn_make_buffer_rsrc((void*)ws, 0, (int)WS_END, 0x00020000);
  const rsrc_t rs_out = __builtin_amdgcn_make_buffer_rsrc((void*)out, 0, L_ * D_ * 2, 0x00020000);
  auto conv_in1 = [&](int tid, int layer, int h, auto N) {
    constexpr int j = N / 3, e = N % 3;
    const u32 vo = tid * 16;
    if constexpr (e == 0) in_su[j] = ld16(rs, vo, (u32)WS_STAT + layer * 32768 + h * 8192 + j * 4096);
    if constexpr (e == 1) in_sq[j] = ld16(rs, vo, (u32)WS_STAT + layer * 32768 + h * 8192 + 16384 + j * 4096);
    if constexpr (e == 2) in_xv[j] = ld16(rs, vo, (u32)WS_XQ0 + layer * (2u << 20) + ((cp >> 1) * L_ + 1024 * h) * 8 + j * 4096);
  };
  u32x4 A0[4][4];
#define LDW1(n) if constexpr (!STATS) wf[n] = ld16(rs, lane * 16 + ((n) & 3) * 1024, o_w1 + ((n) >> 2) * 4096)
#define OW1() const u32 o_w1 = opq((u32)WS_W1P + grp * 49152);
      auto ln = [&](int tid, int h) {
        if (h == 0 && tid < 64) *(LAS u32x4*)(sm + M_U + (tid >> 5) * 4608 + (tid & 31) * 16) = u32x4{0, 0, 0, 0};
        f32x2 mean[2], var[2], ri[2];
#pragma unroll
        for (int j = 0; j < 2; j++) {
          const f64x2 s1 = __builtin_bit_cast(f64x2, in_su[j]), s2 = __builtin_bit_cast(f64x2, in_sq[j]);
          mean[j] = f32x2{(float)s1.x, (float)s1.y} * (1.f / D_);
          var[j] = f32x2{(float)s2.x, (float)s2.y} * (1.f / D_) - mean[j] * mean[j];
        }
#pragma unroll
        for (int j = 0; j < 2; j++) var[j] = unp(pkb(f32x2{fmaxf(var[j].x, 0.f), fmaxf(var[j].y, 0.f)})) + 1e-5f;
#pragma unroll
        for (int j = 0; j < 2; j++) { var[j] = unp(pkb(var[j])); mean[j] = unp(pkb(mean[j])); }
#pragma unroll
        for (int j = 0; j < 2; j++) var[j] = f32x2{__builtin_amdgcn_sqrtf(var[j].x), __builtin_amdgcn_sqrtf(var[j].y)};
        u32 xc[2][2];
        f32x2 d[2][2];
#pragma unroll
        for (int j = 0; j < 2; j++) {
          const u32 p0 = in_xv[j].x, p1 = in_xv[j].y, p2 = in_xv[j].z, p3 = in_xv[j].w;
          const u32 xa = (cp & 1) ? p1 : p0, xb = (cp & 1) ? p3 : p2;
          xc[j][0] = __builtin_amdgcn_perm(xb, xa, 0x05040100u);
          xc[j][1] = __builtin_amdgcn_perm(xb, xa, 0x07060302u);
          d[j][0] = f32x2{bf_lo(xa), bf_lo(xb)} - mean[j];
          d[j][1] = f32x2{bf_hi(xa), bf_hi(xb)} - mean[j];
        }
#pragma unroll
        for (int j = 0; j < 2; j++) { var[j] = unp(pkb(var[j])); ri[j] = f32x2{__builtin_amdgcn_rcpf(var[j].x), __builtin_amdgcn_rcpf(var[j].y)}; }
#pragma unroll
        for (int j = 0; j < 2; j++)
#pragma unroll
          for (int cc = 0; cc < 2; cc++) {
            *(LAS u32*)(sm + M_XS + h * 4096 + cc * 2048 + (tid + 256 * j) * 4) = xc[j][cc];
            *(LAS u32*)(sm + M_U + cc * 4608 + 512 + 2048 * h + (tid + 256 * j) * 4) = pkh(d[j][cc] * ri[j]);
          }
      };
  {
    TIDS()
    OW1()
    ln(tid, 0);
    BAR();
    STAMP(9)
    const int cpy = (3 - r) & 3;
    LAS char* pa = sm + M_KC + (w >> 1) * (4 * KCS) + cpy * KCS + 2 * (15 + 8 * g4 - r - cpy) + 64 * (4 - 4 * (w & 1)) + 2048;
    sfor<16>([&](auto n) {
      constexpr int rr = n / 4, i = n % 4;
      const u32x2 lo = *(LAS u32x2*)(pa + 512 * (3 - i) + 64 * (3 - rr)), hi = *(LAS u32x2*)(pa + 512 * (3 - i) + 64 * (3 - rr) + 8);
      A0[rr][i] = u32x4{lo.x, lo.y, hi.x, hi.y};
      if constexpr (n % 2 == 0 && n < 14) { LDW1(8 + n / 2); }
      if constexpr (!STATS && n % 2 == 1 && n < 6) cs[n / 2] = __builtin_bit_cast(f32x4, ld16(rs, r * 256 + g4 * 16, (u32)WS_CSP + ((grp * 3 + n / 2) >> 2) * 4096 + ((grp * 3 + n / 2) & 3) * 64));
      SB();
    });
  }
#pragma unroll 1
  for (int layer = 0; layer < 2; layer++) {
#define ST(i) STAMP(20 + layer * 100 + 70 + (i))
    const u32 o_xt = opq((u32)WS_XT1 + layer * (2u << 20));
    const u32 o_h = opq((u32)WS_H + layer * (u32)H_BYTES);
    const u32 o_gr = (u32)WS_GRAN + layer * 16384;
    u32x4 A2[43];
    const u32 o_w2 = opq((u32)WS_W2P + db * KS2 * 4096 + w * 1024);
    auto ldw2 = [&](int lane, auto n) { A2[n] = ld16(rs, lane * 16, o_w2 + n * 4096); };
#define LDWC(n) if constexpr (STATS) ldw2(lane, IC<(n) - 15>{}); else wf[n] = ld16(rs, lane * 16 + ((n) & 3) * 1024, o_w1 + ((n) >> 2) * 4096)
    u32 xv0;
    const u32 ldsw = opq((u32)(uintptr_t)sm + w * 1024);
    auto stage = [&](auto H, auto K) { glds<M_X + K * 4096, H * 2048 + 32 * K * L_ * 2>(xv0, rs, ldsw, o_xt); };
    {
      TIDS()
      OW1()
      xv0 = ((8 * w + (lane >> 3)) * L_ + tt * 64 + 16 * (((lane & 7) >> 1) ^ (((lane >> 4) & 1) | ((w & 1) << 1))) + 8 * (lane & 1)) * 2;
      const int c = w >> 1, half = w & 1;
      const int cpy = (3 - r) & 3;
      LAS char* pa = sm + M_KC + c * (4 * KCS) + cpy * KCS + 2 * (15 + 8 * g4 - r - cpy) + 64 * (4 - 4 * half);
      LAS char* pb = sm + M_U + c * 4608 + 2 * (240 + 16 * r + 8 * g4) - 64 * (4 * half + 3);
      u32x4 A1[4][4], B[4][4];
      f32x4 acc[4], accH[4];
      auto rdA = [&](int rr, int i) {
        const u32x2 lo = *(LAS u32x2*)(pa + 512 * (3 - i) + 64 * (3 - rr)), hi = *(LAS u32x2*)(pa + 512 * (3 - i) + 64 * (3 - rr) + 8);
        A1[rr][i] = u32x4{lo.x, lo.y, hi.x, hi.y};
      };
      auto tri = [&](auto H, f32x4 (&ac)[4], auto side) {
        constexpr int h = H;
        sfor<16>([&](auto n) { B[n / 4][n % 4] = *(LAS u32x4*)(pb + 2048 * h + 512 * (n % 4) + 64 * (3 - n / 4)); SB(); });
        sfor<40>([&](auto n) {
          constexpr int rr = n / 10, e = n % 10;
          constexpr int m = e < 4 ? 0 : e < 7 ? 1 : e < 9 ? 2 : 3;
          constexpr int g = e < 4 ? e : e < 7 ? e - 3 : e < 9 ? e - 5 : 3;
          mmH<(h == 0 && n < 4)>(ac[g], A0[rr][g - m], B[rr][m]);
          side(n);
          SB();
        });
      };
      f32x4 kk[2], rv[2];
      u32x2 xr[2];
      u32 o[2][2];
      auto red_wr = [&](f32x4 (&ac)[4], auto P) {
        constexpr int p_ = P, g = p_ & 1;
        const f32x4 lo = ac[g], hi = ac[2 + g];
        if constexpr (p_ < 2) *(LAS f32x4*)(sm + M_CRED + w * 2048 + lane * 16 + g * 1024) = half ? lo : hi;
        else { kk[g] = half ? hi : lo; pin(kk[g]); }
      };
      auto epi_rd = [&](int h, int g) {
        rv[g] = *(LAS f32x4*)(sm + M_CRED + (w ^ 1) * 2048 + lane * 16 + g * 1024);
        xr[g] = *(LAS u32x2*)(sm + M_XS + h * 4096 + c * 2048 + (256 * (2 * half + g) + 16 * r + 4 * g4) * 2);
      };
      auto epi_c = [&](int g, int e) {
        const f32x2 v = e ? f32x2{kk[g].z, kk[g].w} + f32x2{rv[g].z, rv[g].w} : f32x2{kk[g].x, kk[g].y} + f32x2{rv[g].x, rv[g].y};
        o[g][e] = pkb(v * kinv + unp(e ? xr[g].y : xr[g].x));
      };
      const u32 vo_st = (256 * (2 * half + (g4 & 1)) + 16 * r + 4 * (g4 & 2)) * 2 + o_xt + (cp * 2 + c) * L_ * 2;
      auto epi_st = [&](int h) {
        auto e0 = __builtin_amdgcn_permlane16_swap(o[0][0], o[1][0], false, false);
        auto e1 = __builtin_amdgcn_permlane16_swap(o[0][1], o[1][1], false, false);
        st16(rs, vo_st, 2048 * h, u32x4{e0[0], e1[0], e0[1], e1[1]});
      };
      const u32 pvo = tid < 16 ? tid * 128 : OOB;
      const u32 n32 = tid < 16 ? 16 : 0;
      u32 pv;
      ST(1)
      tri(IC<0>{}, acc, [&](auto n) {
        if constexpr (n % 8 == 3) { LDWC(15 + n / 8); }
        if constexpr (n >= 24 && n % 4 == 1) rdA(0, (n - 24) / 4);
        if constexpr (n == P_CHS) { wait_v<(P_CHS + 5) / 8>(); BARX(); sig_all(rs, 3, b, tid, layer); }
      });
      ST(2)
      sfor<64>([&](auto n) {
        constexpr int rr = n / 16, m = 3 - (n % 16) / 4, g = n % 4, s = n / 4, ia = 4 + g - m;
        mmH<(n < 4)>(accH[g], ia < 4 ? A0[rr][ia & 3] : A1[rr][ia & 3], B[rr][m]);
        if constexpr (n >= 4 && n < 8) red_wr(acc, IC<n - 4>{});
        if constexpr (n == 10) { BAR(); epi_rd(0, 0); epi_rd(0, 1); }
        if constexpr (n >= 15 && n < 23 && n % 2 == 1) epi_c((n - 15) / 4, ((n - 15) / 2) & 1);
        if constexpr (n == 23) epi_st(0);
        if constexpr (n % 4 == 0 && rr < 3 && n > 0) rdA((n - 4) / 16 + 1, (s - 1) % 4);
        if constexpr (n == 48) rdA(3, 3);
        if constexpr (n >= 24 && n % 5 == 1 && n < 46) { LDWC(20 + (n - 26) / 5); }
        if constexpr (n == P_CHP) pv = poll_issue(rs, pvo, po_all(3, b));
        if constexpr (n == P_CHC) { poll_check(rs, pvo, po_all(3, b), pv, layer ? n32 : 0 SL(layer * 20 + 1)); BARX(); }
        if constexpr (n > P_CHC && (n - P_CHC) % 2 == 1 && (n - P_CHC) / 2 < 6) conv_in1(tid, layer, 1, IC<(n - P_CHC) / 2>{});
        SB();
      });
      static_assert(P_CHC + 11 < 64 && P_CHC >= 42 && P_CHP < 23, "");
      ST(3)
      sfor<4>([&](auto n) { LDWC(24 + n); });
      wait_v<14>();
      BARX();
      sig_all(rs, layer * 4, b, tid, true);
      sfor<4>([&](auto n) { LDWC(28 + n); });
      SB();
      ST(4)
      ln(tid, 1);
      BAR();
      ST(5)
      tri(IC<1>{}, accH, [&](auto n) {
        if constexpr (n % 6 == 3 && n < 24) { LDWC(32 + n / 6); }
        if constexpr (n == P_ALP) pv = poll_issue(rs, pvo, po_all(layer * 4, b));
      });
      MM_DONE();
      sfor<2>([&](auto P) { red_wr(accH, P); });
      poll_check(rs, pvo, po_all(layer * 4, b), pv, n32 SL(layer * 20 + 2));
      BAR();
      sfor<2>([&](auto k) { stage(IC<0>{}, k); });
      epi_rd(1, 0); epi_rd(1, 1);
      ST(6)
      sfor<2>([&](auto k) { stage(IC<0>{}, IC<2 + k>{}); });
      sfor<2>([&](auto P) { red_wr(accH, IC<2 + P>{}); });
      SB();
      epi_c(0, 0); epi_c(0, 1); epi_c(1, 0); epi_c(1, 1);
      epi_st(1);
      SB();
      sfor<4>([&](auto k) { stage(IC<0>{}, IC<4 + k>{}); });
      ST(7)
    }
    u32x4 res;
    const u32 o_hr = opq(o_h + (tb * 2 + (w & 1)) * (NT1 * 256) + (w >> 1) * 1024);
    u32 hv0;
    auto sth = [&](auto H, auto J) { glds<M_H + J * 4096, H * 64 * (NT1 * 256) + J * 2048>(hv0, rs, ldsw, o_hr); };
    auto ldres = [&](int tid, int h) { res = ld16(rs, ((tid >> 2) * L_ + (tid & 3) * 8) * 2, o_xt + (db * 64 * L_ + 1024 * h + tb * 32) * 2); };
    {
      TIDS()
      hv0 = lane * 16;
      const int q = r >> 2, pp = lane & 3;
      const int fsw = ((q >> 1) & 1) | ((g4 & 1) << 1);
      const u32 pvo = tid < 16 ? tid * 128 : OOB, pv0 = tid == 0 ? 0 : OOB, n32 = tid < 16 ? 16 : 0, n15 = tid == 0 ? 15 : 0;
      const u32 pah = po_all(layer * 4 + 1, b), pbl = po_loc((layer * 2) * 16 + (tb >> 1));
      if constexpr (STATS) {
        sfor<8>([&](auto k) { stage(IC<0>{}, IC<8 + k>{}); });
        LAS char* xr = sm + M_X + (8 * g4 + q) * 128 + pp * 8 + 32 * (w ^ fsw);
        u32 pv;
        sfor<2>([&](auto H) {
          constexpr int h = H;
          const u32x4 ones = {0x3f803f80u, 0x3f803f80u, 0x3f803f80u, 0x3f803f80u};
          f32x4 as = {0.f, 0.f, 0.f, 0.f}, aq = as;
          sfor<2>([&](auto KK) {
            wait_v<KK == 0 ? 8 : h == 0 ? 1 : 0>();
            BARX();
            if constexpr (h == 0 && KK == 0) sig_all(rs, layer * 4 + 1, b, tid, true);
            u32x2 lo[8], hi[8];
#pragma unroll
            for (int k = 0; k < 8; k++) { lo[k] = tr_read(xr + (8 * KK + k) * 4096); hi[k] = tr_read(xr + (8 * KK + k) * 4096 + 512); }
            SB();
#pragma unroll
            for (int k = 0; k < 8; k++) {
              const u32x4 fr = {lo[k].x, lo[k].y, hi[k].x, hi[k].y};
              as = mfma_bf16(ones, fr, as);
              aq = mfma_bf16(fr, fr, aq);
            }
            pin(as); pin(aq);
          });
          const float ssq = pp == 0 ? aq[0] : pp == 1 ? aq[1] : pp == 2 ? aq[2] : aq[3];
          const float mean = as[0] * (1.f / D_);
          const float var = fmaxf(ssq * (1.f / D_) - mean * mean, 0.f);
          const float sd = __builtin_amdgcn_sqrtf(rbf(rbf(var) + 1e-5f));
          st8(rs, q == g4 ? (w * 16 + r) * 8 : OOB, o_gr + (1024 * h + tt * 64) * 8, u32x2{pk_bf16(mean, sd), GTAG});
          SB();
          if constexpr (h == 0) {
            sfor<10>([&](auto n) { ldw2(lane, IC<21 + n>{}); });
            SB();
            pv = poll_issue(rs, pvo, pah);
            poll_check(rs, pvo, pah, pv, n32 SL(layer * 20 + 3));
            BAR();
            sfor<16>([&](auto k) { stage(IC<1>{}, k); });
          }
        });
        sfor<7>([&](auto n) { ldw2(lane, IC<31 + n>{}); });
        ldres(tid, 0);
        pv = poll_issue(rs, pv0, pbl);
        poll_check(rs, pv0, pbl, pv, n15 SL(layer * 20 + 4));
        BAR();
        sfor<22>([&](auto j) { sth(IC<0>{}, j); });
      } else {
        f32x4 acc[4][3];
        u32x2 gr[4];
        u32 hvo[3];
#pragma unroll
        for (int nn = 0; nn < 3; nn++) {
          const int item = 4 * nn + g4, ii = item / 3, jj = item - 3 * ii;
          hvo[nn] = ii * (NT1 * 256) + jj * 256 + r * 16 + ((grp * 3 + jj < NT1) ? 0u : OOB) + o_h + tt * 4 * (NT1 * 256) + grp * 768;
        }
        u32 pv;
        auto kloop = [&](auto H, auto side) {
          constexpr int h = H;
          LAS char* xr = sm + M_X + (8 * g4 + q) * 128 + pp * 8;
          u32x2 lo[2][4], hi[2][4];
          sfor<4>([&](auto i) { lo[0][i] = tr_read(xr + 32 * (i ^ fsw)); hi[0][i] = tr_read(xr + 32 * (i ^ fsw) + 512); SB(); });
          sfor<64>([&](auto n) {
            constexpr int k = n / 4, i = n % 4;
            const u32x4 xf = {lo[k & 1][i].x, lo[k & 1][i].y, hi[k & 1][i].x, hi[k & 1][i].y};
            side(IC<2 * n>{});
            SB();
            mmA<k == 0>(acc[i][0], wf[k * 3], xf);
            if constexpr (k < 15) lo[(k + 1) & 1][i] = tr_read(xr + (k + 1) * 4096 + 32 * (i ^ fsw));
            SB();
            mmA<k == 0>(acc[i][1], wf[k * 3 + 1], xf);
            if constexpr (k < 15) hi[(k + 1) & 1][i] = tr_read(xr + (k + 1) * 4096 + 32 * (i ^ fsw) + 512);
            SB();
            mmA<k == 0>(acc[i][2], wf[k * 3 + 2], xf);
            side(IC<2 * n + 1>{});
            SB();
          });
          MM_DONE();
        };
        auto ldgr = [&](int h, int i) { gr[i] = __builtin_amdgcn_raw_buffer_load_b64(rs, r * 8 + i * 128, o_gr + (1024 * h + tt * 64) * 8, AUX_SC1); };
        auto epi1 = [&](int h, auto side) {
#define GR_OK() __all((gr[0].y == GTAG) & (gr[1].y == GTAG) & (gr[2].y == GTAG) & (gr[3].y == GTAG))
          if (__builtin_expect(!GR_OK(), 0)) {
            SL_BEGIN()
#pragma unroll 1
            for (int spin = 0; spin < SPIN_CAP; spin++) {
              asm volatile("" ::: "memory");
              ldgr(h, 0); ldgr(h, 1); ldgr(h, 2); ldgr(h, 3);
              if (GR_OK()) break;
            }
            SL_END(layer * 20 + 15 + h)
          }
          u32 hv[12];
          sfor<4>([&](auto I) {
            constexpr int i = I;
            const float m = bf_lo(gr[i].x), sd = bf_hi(gr[i].x);
            const float kx = -1.44269504088896341f * __builtin_amdgcn_rcpf(sd), cd = -1.44269504088896341f * sd;
            f32x2 y[3], au[3], e[3];
#pragma unroll
            for (int j = 0; j < 3; j++) y[j] = f32x2{acc[i][j].x, acc[i][j].y} - m * f32x2{cs[j].x, cs[j].y};
#pragma unroll
            for (int j = 0; j < 3; j++) { y[j] *= kx; au[j] = f32x2{acc[i][j].z, acc[i][j].w} - m * f32x2{cs[j].z, cs[j].w}; }
#pragma unroll
            for (int j = 0; j < 3; j++) e[j] = f32x2{__builtin_amdgcn_exp2f(y[j].x), __builtin_amdgcn_exp2f(y[j].y)};
            side(IC<3 * i>{});
#pragma unroll
            for (int j = 0; j < 3; j++) { e[j] = e[j] * cd + cd; y[j] *= au[j]; }
            side(IC<3 * i + 1>{});
#pragma unroll
            for (int j = 0; j < 3; j++) e[j] = f32x2{__builtin_amdgcn_rcpf(e[j].x), __builtin_amdgcn_rcpf(e[j].y)};
            side(IC<3 * i + 2>{});
#pragma unroll
            for (int j = 0; j < 3; j++) hv[3 * i + j] = pkb(y[j] * e[j]);
            if constexpr (i >= 1) {
              constexpr int nn = i - 1;
              auto s02 = __builtin_amdgcn_permlane32_swap(hv[4 * nn], hv[4 * nn + 2], false, false);
              auto s13 = __builtin_amdgcn_permlane32_swap(hv[4 * nn + 1], hv[4 * nn + 3], false, false);
              auto a01 = __builtin_amdgcn_permlane16_swap(s02[0], s13[0], false, false);
              auto a23 = __builtin_amdgcn_permlane16_swap(s02[1], s13[1], false, false);
              st16(rs, hvo[nn], h * 64 * (NT1 * 256), u32x4{a01[0], a01[1], a23[0], a23[1]});
            }
            SB();
          });
        };
        OW1()
        if (layer == 0) {
#pragma unroll
          for (int j = 0; j < 3; j++)
#pragma unroll
            for (int e = 0; e < 4; e++) { float v = cs[j][e]; DPP_ADD(v, 0x128); DPP_ADD(v, 0x124); DPP_ADD(v, 0x122); DPP_ADD(v, 0x121); cs[j][e] = v; }
        }
        wait_v<5>();
        BARX();
        kloop(IC<0>{}, [&](auto n2) {
          constexpr int k = n2 / 8, i = (n2 / 2) % 4, post = n2 & 1;
          if constexpr (!post && i == 0 && k == 3) { wait_v<9>(); BARX(); sig_all(rs, layer * 4 + 1, b, tid, true); }
          if constexpr (!post && i == 0 && k == 7) { wait_v<10>(); BARX(); }
          if constexpr (!post && i == 0 && k == 11) { wait_v<7>(); BARX(); }
          if constexpr (post && i == 0 && k < 8) stage(IC<0>{}, IC<8 + k>{});
          if constexpr (post && (i & 1) && k < 6) { LDW1(36 + 2 * k + (i >> 1)); }
          if constexpr (post && i == 2 && k >= 6 && k < 14) ldw2(lane, IC<(k < 8 ? k - 6 : 2 * k - 14)>{});
          if constexpr (post && i == 0 && k >= 8 && k < 14) ldw2(lane, IC<2 * k - 13>{});
          if constexpr (post && k == P_GRL && (i & 1)) { ldgr(0, 2 * (i >> 1)); ldgr(0, 2 * (i >> 1) + 1); }
          if constexpr (post && i == 1 && k == 11) pv = poll_issue(rs, pvo, pah);
        });
        ST(8)
        poll_check(rs, pvo, pah, pv, n32 SL(layer * 20 + 5));
        BARX();
        ST(9)
        epi1(0, [&](auto n) {
          if constexpr (n < 4) { stage(IC<1>{}, IC<2 * n>{}); stage(IC<1>{}, IC<2 * n + 1>{}); }
          else stage(IC<1>{}, IC<4 + n>{});
        });
        ST(10)
        wait_v<15>();
        BAR();
        kloop(IC<1>{}, [&](auto n2) {
          constexpr int k = n2 / 8, i = (n2 / 2) % 4, post = n2 & 1;
          if constexpr (!post && i == 0 && k == 3) { wait_v<9 + 4>(); BARX(); }
          if constexpr (!post && i == 0 && k == 5) { wait_v<8>(); BARX(); sig_loc(rs, (layer * 2) * 16 + tt, tid, true); }
          if constexpr (post && !(i & 1) && k >= 1 && k < 13) ldw2(lane, IC<12 + 2 * k + (i >> 1)>{});
          if constexpr (post && i == 1 && k == P_BLP) pv = poll_issue(rs, pv0, pbl);
          if constexpr (post && k == 10 && (i & 1)) { ldgr(1, 2 * (i >> 1)); ldgr(1, 2 * (i >> 1) + 1); }
          if constexpr (post && i == 3 && k == 9) ldres(tid, 0);
          if constexpr (!post && i == 0 && k == 14) { pin(gr[3]); poll_check(rs, pv0, pbl, pv, n15 SL(layer * 20 + 6)); BARX(); }
          if constexpr (post && i < 3 && k >= 14) sth(IC<0>{}, IC<3 * (k - 14) + i>{});
        });
        ST(11)
        BARX();
        ST(12)
        epi1(1, [&](auto n) {
          if constexpr (n < 4) { sth(IC<0>{}, IC<6 + 2 * n>{}); sth(IC<0>{}, IC<7 + 2 * n>{}); }
          else sth(IC<0>{}, IC<10 + n>{});
        });
        ST(13)
      }
    }
    {
      TIDS()
      OW1()
      f32x4 acc[2];
      auto res_init = [&]() {
        *(LAS u32x4*)(sm + M_RES + (tid >> 2) * 80 + (tid & 3) * 16) = res;
#pragma unroll
        for (int i = 0; i < 2; i++) {
          const u32x2 xr = tr_read(sm + M_RES + (16 * w + 4 * g4 + (r >> 2)) * 80 + i * 32 + (lane & 3) * 8);
          acc[i] = f32x4{bf_lo(xr.x), bf_hi(xr.x), bf_lo(xr.y), bf_hi(xr.y)};
          pin(acc[i]);
        }
        SB();
      };
      auto mm2 = [&](auto side) {
        constexpr int PD = 6;
        u32x4 Bf[8];
        LAS char* hb = sm + M_H + lane * 16;
        LAS char* hb2 = hb + 32768;
        auto rdh = [&](auto n) { Bf[n % 8] = n < 32 ? *(LAS u32x4*)(hb + n * 1024) : *(LAS u32x4*)(hb2 + (n - 32) * 1024); };
        sfor<PD>([&](auto n) { rdh(n); SB(); });
        sfor<86>([&](auto n) {
          side(IC<2 * n>{});
          SB();
          if constexpr (n / 2 < 10) mmV(acc[n & 1], A2[n / 2], Bf[n % 8]);
          else mmA<false>(acc[n & 1], A2[n / 2], Bf[n % 8]);
          if constexpr (n + PD < 86) rdh(IC<n + PD>{});
          side(IC<2 * n + 1>{});
          SB();
        });
        MM_DONE();
      };
      const u32 ovo = layer ? (r * D_ + 4 * g4) * 2 : (g4 * L_ + r) * 8;
      auto epi2 = [&](int h) {
        float s1[2], s2[2];
#pragma unroll
        for (int i = 0; i < 2; i++) {
          const u32 o0 = pkb(f32x2{acc[i].x, acc[i].y}), o1 = pkb(f32x2{acc[i].z, acc[i].w});
          st8(layer ? rs_out : rs, ovo,
              layer ? ((1024 * h + tb * 32 + 16 * i) * D_ + db * 64 + 16 * w) * 2 : (u32)WS_XQ2 + ((db * 16 + 4 * w) * L_ + 1024 * h + tb * 32 + 16 * i) * 8, u32x2{o0, o1});
          const float a = bf_lo(o0), bq = bf_hi(o0), cq = bf_lo(o1), dq = bf_hi(o1);
          s1[i] = (a + bq) + (cq + dq);
          s2[i] = (a * a + bq * bq) + (cq * cq + dq * dq);
        }
        auto x0 = __builtin_amdgcn_permlane16_swap(__float_as_uint(s1[0]), __float_as_uint(s2[0]), false, false);
        auto x1 = __builtin_amdgcn_permlane16_swap(__float_as_uint(s1[1]), __float_as_uint(s2[1]), false, false);
        const float t0 = __uint_as_float(x0[0]) + __uint_as_float(x0[1]), t1 = __uint_as_float(x1[0]) + __uint_as_float(x1[1]);
        auto x2 = __builtin_amdgcn_permlane32_swap(__float_as_uint(t0), __float_as_uint(t1), false, false);
        const float t2 = __uint_as_float(x2[0]) + __uint_as_float(x2[1]);
        buf_fadd_f64((double)t2, rs, layer ? OOB : (g4 & 1) * 16384 + ((g4 >> 1) * 16 + r) * 8, (u32)WS_STAT + 32768 + (1024 * h + tb * 32) * 8, 0);
        SB();
      };
      const u32 pbh = po_loc((layer * 2 + 1) * 16 + (tb >> 1)), pvo = tid < 16 ? tid * 128 : OOB, pv0 = tid == 0 ? 0 : OOB;
      u32 pv;
      res_init();
      wait_v<STATS ? 12 : 15>();
      BARX();
      mm2([&](auto n2) {
        constexpr int n = n2 / 2, post = n2 & 1, j = n / 4;
        if constexpr (post && n % 4 == 1 && j < 5) ldw2(lane, IC<38 + j>{});
        if constexpr (post && n % 4 == 1 && j == 5) ldres(tid, 1);
        if constexpr (post && n % 4 == 1 && j >= 6 && j < 21) { LDW1(j - 6); }
        if constexpr (!post && n == 32) { wait_v<STATS ? 3 + 6 : 5 + 8>(); BARX(); }
        if constexpr (!post && n == P_BHS) { wait_v<STATS ? 6 : P_BHS / 4>(); BARX(); if constexpr (!STATS) sig_loc(rs, (layer * 2 + 1) * 16 + tt, tid, true); }
        if constexpr (post && n == P_BHP) pv = poll_issue(rs, pv0, pbh);
      });
      ST(14)
      epi2(0);
      res_init();
      ST(15)
      poll_check(rs, pv0, pbh, pv, tid == 0 ? 15 : 0 SL(layer * 20 + 7));
      BARX();
      sfor<HPRE>([&](auto j) { sth(IC<1>{}, j); });
      ST(17)
      wait_v<HPRE - 4>();
      BARX();
      sig_all(rs, 2, b, tid, !layer);
      mm2([&](auto n2) {
        constexpr int n = n2 / 2, post = n2 & 1, j = n / 4;
        if constexpr (post && n % 4 == 1 && j + HPRE < 22) sth(IC<1>{}, IC<HPRE + j>{});
        if constexpr (!post && (n + 6) % 12 == 4 && n >= 10 && n < 80) {
          constexpr int g = (n + 6) / 4, issued = HPRE + j + 1 < 22 ? HPRE + j + 1 : 22, need = g + 3 < 22 ? g + 3 : 22;
          wait_v<issued - need>();
          if constexpr (g == 19) poll_check(rs, pvo, po_all(2, b), pv, (tid < 16 && !layer) ? 16 : 0 SL(layer * 20 + 8));
          BARX();
        }
        if constexpr (post && n == P_CLP) pv = poll_issue(rs, layer ? OOB : pvo, po_all(2, b));
        if constexpr (post && n >= 72 && n % 2 == 0 && n < 84) conv_in1(tid, 1, 0, IC<(n - 72) / 2>{});
      });
      ST(18)
      epi2(1);
      ST(19)
      if (layer) break;
      ln(tid, 0);
      BAR();
    }
  }
  STAMP_END()
}

constexpr int P_WT = M_W;
constexpr int P_VT = P_WT + 2 * 32 * 528;
constexpr int P_T = P_VT + 2 * 32 * 528;
constexpr int P_TR = P_T + 2 * 648 * 2;
static_assert(P_TR + 4 * 32 * 144 <= M_LDS && P_TR % 16 == 0, "");

constexpr int c_rne(int l) {
  int sh = 0;
  while ((l >> sh) >= 256) sh++;
  if (sh == 0) return l;
  const int q = l >> sh, rem = l & ((1 << sh) - 1), half = 1 << (sh - 1);
  return (q + ((rem > half || (rem == half && (q & 1))) ? 1 : 0)) << sh;
}
constexpr int c_idx(int lam) { int sh = 0; while ((lam >> sh) >= 256) sh++; return sh == 0 ? lam : 128 * sh + (lam >> sh); }
struct Tabs { u16 g[256][16]; u32 t[128][8]; };
constexpr Tabs make_tabs() {
  Tabs x{};
  for (int t = 0; t < 256; t++)
    for (int e = 0; e < 16; e++) { const int l = 2047 - (8 * t + e); x.g[t][e] = (u16)(2 * (l < 0 ? 645 : c_idx(c_rne(l)))); }
  for (int i = 0; i < 128; i++)
    for (int j = 0; j < 8; j++) {
      const int col = 16 * (i >> 6) + (i & 15), row = 16 * (j >> 2) + 4 * ((i >> 4) & 3) + (j & 3);
      const int al = row < 8 ? row : row < 12 ? 256 + 2 * (row - 8) : row < 16 ? 512 + 256 * ((row - 12) >> 1) + 4 * (row & 1) : 1024 + 256 * (row - 16);
      const int lam = al + 8 * col;
      const bool ok = row < 20 || (row == 20 && col == 0);
      x.t[i][j] = ok ? ((u32)lam << 16) | (u32)(2 * c_idx(lam)) : (u32)(2 * 644) | (1u << 16);
    }
  return x;
}
__device__ const Tabs g_tabs __attribute__((aligned(16))) = make_tabs();

__global__ void __launch_bounds__(256, 1) k_main(P0 p) {
  extern __shared__ __attribute__((aligned(16))) char smem_[];
  LAS char* sm = (LAS char*)smem_;
  char* ws = p.ws;
  const int b = blockIdx.x, tid = threadIdx.x, lane = tid & 63, w = __builtin_amdgcn_readfirstlane(tid >> 6);
  const int r = lane & 15, g4 = lane >> 4;
  const rsrc_t rs = __builtin_amdgcn_make_buffer_rsrc((void*)ws, 0, (int)WS_END, 0x00020000);
  STAMP_DECL(1, ws)
  constexpr int CODE_BYTES = 49152;
  const u32 code_pf = __builtin_amdgcn_raw_buffer_load_b32(
      __builtin_amdgcn_make_buffer_rsrc((void*)(__builtin_amdgcn_s_getpc() & ~127ull), 0, CODE_BYTES, 0x00020000),
      tid < 12 ? ((b >> 3) * 12 + tid) * 128 : OOB, 0, 0);
  SB();
  auto wt_load = [&](int task, u32x4 (&v)[4]) {
    const bool up = task < 688;
    const int t2 = task - 688;
    const u32 so = up ? ((task & 15) * 32 * F_ + (task >> 4) * 32) * 2 : ((t2 >> 3) * 32 * D_ + (t2 & 7) * 64) * 2;
    const u32 ld = up ? F_ * 2 : D_ * 2;
    const rsrc_t r0 = __builtin_amdgcn_make_buffer_rsrc((void*)(up ? p.wg : p.wd), 0, D_ * F_ * 2, 0x00020000);
    const rsrc_t r1 = __builtin_amdgcn_make_buffer_rsrc((void*)(up ? p.wu : p.wd), 0, D_ * F_ * 2, 0x00020000);
    const u32 vo = (lane >> 2) * ld + (lane & 3) * 16, s1 = so + (up ? 0 : 64);
    v[0] = __builtin_amdgcn_raw_buffer_load_b128(r0, vo, so, 0); v[1] = __builtin_amdgcn_raw_buffer_load_b128(r1, vo, s1, 0);
    v[2] = __builtin_amdgcn_raw_buffer_load_b128(r0, vo, so + 16 * ld, 0); v[3] = __builtin_amdgcn_raw_buffer_load_b128(r1, vo, s1 + 16 * ld, 0);
  };
  auto wt_proc = [&](int task, u32x4 (&v)[4], u32x4& csv) {
    LAS char* tr = sm + P_TR + w * (32 * 144);
    if (task < 688) {
#pragma unroll
      for (int i = 0; i < 2; i++) {
        LAS char* dst = tr + (i * 16 + (lane >> 2)) * 144 + (lane & 3) * 32;
        *(LAS u32x4*)dst = u32x4{v[2 * i].x, v[2 * i + 1].x, v[2 * i].y, v[2 * i + 1].y};
        *(LAS u32x4*)(dst + 16) = u32x4{v[2 * i].z, v[2 * i + 1].z, v[2 * i].w, v[2 * i + 1].w};
      }
    } else {
#pragma unroll
      for (int i = 0; i < 4; i++) *(LAS u32x4*)(tr + ((i >> 1) * 16 + (lane >> 2)) * 144 + (i & 1) * 64 + (lane & 3) * 16) = v[i];
    }
    f32x4 cs = {0.f, 0.f, 0.f, 0.f};
#pragma unroll
    for (int i = 0; i < 4; i++) {
      LAS char* a = tr + (8 * g4 + (r >> 2)) * 144 + i * 32 + (lane & 3) * 8;
      const u32x2 lo = tr_read(a), hi = tr_read(a + 4 * 144);
      v[i] = u32x4{lo.x, lo.y, hi.x, hi.y};
      const u32 one = r == i ? 0x3f803f80u : 0u;
      cs = mfma_bf16(v[i], u32x4{one, one, one, one}, cs);
    }
    csv = __builtin_bit_cast(u32x4, cs);
  };
  auto wt_store = [&](int task, u32x4 (&v)[4], const u32x4& csv, int i) {
    const bool up = task < 688;
    if (i == 4) {
      st16(rs, (r < 4 && up) ? r * 64 + g4 * 16 : OOB, (u32)WS_CSP + task * 256, csv);
    } else {
      const int t2 = task - 688, tw = (task >> 4) * 4 + i, g3 = (tw * 43691) >> 17;
      const u32 o1 = (u32)WS_W1P + (g3 * 45 + (task & 15) * 3 + tw) * 1024, o2 = (u32)WS_W2P + (((t2 & 7) * KS2 + (t2 >> 3)) * 4 + i) * 1024;
      st16(rs, lane * 16, up ? o1 : o2, v[i]);
    }
    SB();
  };
  const int gw0 = b * 4 + w, ksq = (gw0 * 1525) >> 16;
  const int gw = gw0 < 688 ? (gw0 - ksq * 43) * 16 + ksq : gw0;
  u32x4 wv[4], xin[2], xs[2], csv;
  const u32 epoch = __builtin_amdgcn_raw_buffer_load_b32(rs, 0, (u32)WS_EPOCH, AUX_SC1);
  {
    const rsrc_t rx = __builtin_amdgcn_make_buffer_rsrc((void*)p.x0, 0, L_ * D_ * 2, 0x00020000);
#pragma unroll
    for (int i = 0; i < 2; i++) {
      xin[i] = __builtin_amdgcn_raw_buffer_load_b128(rx, (tid & 7) * 2048 + (tid >> 3) * 16 + i * 1024, (b >> 1) * 16384 + (b & 1) * 512, 0);
      xs[i] = __builtin_amdgcn_raw_buffer_load_b128(rx, lane * 16 + i * 1024, b * 8192 + w * 2048, 0);
    }
  }
  SB();
  const int cp = ((b >> 4) * 8 + (b & 7)) * 2 + ((b >> 3) & 1);
  const int c = w >> 1, kind = w & 1, d = cp * 2 + c;
  const int pi = d * N_ + 2 * lane;
  const u32 are = *(const u32*)(p.are + pi), aim = *(const u32*)(p.aim + pi);
  const u32 bre = *(const u32*)(p.bre + pi), bim = *(const u32*)(p.bim + pi), cre = *(const u32*)(p.cre + pi), cim = *(const u32*)(p.cim + pi);
  const u32 ldt = *(const u32*)(p.logdt + cp * 2), dsk = *(const u32*)(p.dskip + cp * 2);
  SB();
  wt_load(gw, wv);
  SB();
  __builtin_amdgcn_raw_buffer_store_b32(0u, rs, (tid < 7 && b * 7 + tid < CNT_LINES) ? (b * 7 + tid) * 128 : OOB, (u32)WS_CNT, AUX_SC1);
  st16(rs, tid < 8 ? (tid >> 2) * 16384 + (tid & 3) * 16 : OOB, (u32)WS_STAT + 32768 + b * 64, u32x4{0, 0, 0, 0});
  if (tid < 16) *(LAS u32x4*)(sm + M_KC + (tid >> 1) * KCS + 4096 + (tid & 1) * 16) = u32x4{0, 0, 0, 0};
  if (tid < 2) *(LAS u32*)(sm + P_T + tid * 1296 + 1288) = 0u;
  SB();
  const u32 tag = __builtin_amdgcn_readfirstlane(epoch) * 0x9E3779B1u + 0x7F4A7C15u;
  st8(rs, tid < 2 ? tid * 32768 : OOB, (u32)WS_TAGS + b * 128, u32x2{0u, tag});
  STAMP(1)
  {
    const u32 vo = (((b & 1) * 64 + (tid >> 3) * 2) * L_ + (b >> 1) * 16 + 2 * (tid & 7)) * 8 + (u32)WS_XQ0;
    st16(rs, vo, 0, u32x4{xin[0].x, xin[0].y, xin[1].x, xin[1].y});
    st16(rs, vo, L_ * 8, u32x4{xin[0].z, xin[0].w, xin[1].z, xin[1].w});
    float st[4];
#pragma unroll
    for (int i = 0; i < 2; i++) {
      f32x2 s1 = {0.f, 0.f}, s2 = s1;
#pragma unroll
      for (int e = 0; e < 4; e++) { const f32x2 x = unp(xs[i][e]); s1 += x; s2 += x * x; }
      st[i] = s1.x + s1.y; st[2 + i] = s2.x + s2.y;
    }
    auto a0 = __builtin_amdgcn_permlane16_swap(__float_as_uint(st[0]), __float_as_uint(st[2]), false, false);
    auto a1 = __builtin_amdgcn_permlane16_swap(__float_as_uint(st[1]), __float_as_uint(st[3]), false, false);
    float v0 = __uint_as_float(a0[0]) + __uint_as_float(a0[1]), v1 = __uint_as_float(a1[0]) + __uint_as_float(a1[1]);
    auto a2 = __builtin_amdgcn_permlane32_swap(__float_as_uint(v0), __float_as_uint(v1), false, false);
    float v = __uint_as_float(a2[0]) + __uint_as_float(a2[1]);
    DPP_ADD(v, 0x128); DPP_ADD(v, 0x124); DPP_ADD(v, 0x122); DPP_ADD(v, 0x121);
    st8(rs, r == 0 ? (g4 & 1) * 16384 + (g4 >> 1) * 8 : OOB, (u32)WS_STAT + b * 64 + w * 16, __builtin_bit_cast(u32x2, (double)v));
  }
  SB();
  const u32x4 tt0 = *(const u32x4*)(&g_tabs.t[kind * 64 + lane][0]), tt1 = *(const u32x4*)(&g_tabs.t[kind * 64 + lane][4]);
  const u32x4 gt0 = *(const u32x4*)(&g_tabs.g[tid][0]);
  const u32x2 gt1 = *(const u32x2*)(&g_tabs.g[tid][8]);
  SB();
  STAMP(2)
  const float S = 16384.f, LOG2E = 1.44269504088896341f;
  LAS char* Wt = sm + P_WT + c * (32 * 528);
  LAS char* Vt = sm + P_VT + c * (32 * 528);
  const float dt = rbf(expf(c ? bf_hi(ldt) : bf_lo(ldt)));
  f32x2 zl = unp(are) * (dt * LOG2E);
  const f32x2 zt = unp(aim) * dt * 0.159154943091895336f;
  cx2 cc = cmul(cx2{unp(cre), unp(cim)}, cx2{unp(bre) * dt, unp(bim) * dt});
  float mu = fmaxf(zl.x, zl.y);
  float cm = fmaxf(fmaxf(fmaxf(fabsf(cc.re.x), fabsf(cc.re.y)), fmaxf(fabsf(cc.im.x), fabsf(cc.im.y))), 1e-30f);
  wave_max2(mu, cm);
  zl -= mu;
#define PUT(dst, row, v) *(LAS u32x2*)((dst) + (row) * 528 + lane * 8) = u32x2{pkh((v).re), pkh((v).im)}
  const cx2 st8c = cexp2(zl, -zt, 8.f, 0.f);
  cx2 eb = cexp2(zl, -zt, kind ? 192.f : 64.f, 14.f);
  cx2 ec = cexp2(zl, kind ? zt : -zt, kind ? 256.f : 128.f, kind ? 0.f : 14.f);
  const cx2 e1 = cexp2(zl, zt, 1.f, 0.f);
  const float dk = c ? bf_hi(dsk) : bf_lo(dsk);
  const float bound = cm * 182.f * __builtin_amdgcn_exp2f(fminf(fmaxf(mu * 2048.f, 0.f), 60.f)) + fabsf(dk);
  const float osc = 32768.f * __builtin_amdgcn_rcpf(bound);
  const float kinv = __builtin_amdgcn_rcpf(osc);
  pin(eb.re); pin(eb.im); pin(ec.re); pin(ec.im);
  asm volatile("" ::"v"(code_pf), "v"(e1.re), "v"(e1.im), "v"(st8c.re), "v"(st8c.im), "v"(kinv));
  STAMP(3)
  wt_proc(gw, wv, csv);
  wait_v<4>();
  BARX();
  const u32x2 ar1 = tc_arrive(rs, 0, b, tid);
#pragma unroll
  for (int i = 0; i < 5; i++) wt_store(gw, wv, csv, i);
  const bool extra = (b & 0xf1) == 0xe1 && w == 0;
  const int xt = 1024 + ((b >> 1) & 7);
  u32x4 wv2[4];
  if (extra) wt_load(xt, wv2);
  STAMP(4)
  if (kind == 0) {
    cx2 ea = cx2{f32x2{S, S}, f32x2{0.f, 0.f}};
#pragma unroll
    for (int j = 0; j < 8; j++) {
      PUT(Vt, j, ea); PUT(Vt, 8 + j, eb); PUT(Vt, 16 + j, ec);
      if (j < 7) { ea = cmul(ea, st8c); eb = cmul(eb, st8c); ec = cmul(ec, st8c); }
    }
  } else {
    const float sc = S * __builtin_amdgcn_rcpf(cm);
    cc.re *= sc; cc.im *= sc;
    const cx2 e2 = cmul(e1, e1), e4 = cmul(e2, e2);
    cx2 c1 = cmul(cc, ec), c2 = c1;
#pragma unroll
    for (int j = 0; j < 8; j++) {
      PUT(Wt, j, cc);
      PUT(Vt, 24 + j, eb);
      if (j < 4) PUT(Wt, 8 + j, c1);
      if (j < 7) { cc = cmul(cc, e1); c2 = cmul(c2, ec); eb = cmul(eb, st8c); }
      if (j < 3) c1 = cmul(c1, e2);
      if (j < 2) { PUT(Wt, 12 + 2 * j, c2); const cx2 d1 = cmul(c2, e4); PUT(Wt, 13 + 2 * j, d1); }
      else if (j < 7) PUT(Wt, 14 + j, c2);
    }
  }
#undef PUT
  tc_arrive_check(rs, 0, b, tid, ar1, tag);
  STAMP(5)
  wait_v<0>();
  BAR();
  const u32x2 ar2 = tc_arrive(rs, 1, b, tid);
  const u32x2 p1 = tc_poll(rs, 0, b, tid);
  {
    LAS char* T = sm + P_T + c * 1296;
    LAS char* pa = Wt + r * 528 + (g4 & 1) * 256 + (g4 >> 1) * 128;
    LAS char* pb = Vt + (16 * kind + r) * 528 + (g4 & 1) * 256 + (g4 >> 1) * 128;
    u32x4 fa[2][8], fb[8];
#pragma unroll
    for (int s = 0; s < 8; s++) { fb[s] = *(LAS u32x4*)(pb + s * 16); fa[0][s] = *(LAS u32x4*)(pa + s * 16); fa[1][s] = *(LAS u32x4*)(pa + 16 * 528 + s * 16); }
    float fac[8];
    const float f0 = cm * (1.f / (S * S)) * osc;
#pragma unroll
    for (int j = 0; j < 8; j++) fac[j] = __builtin_amdgcn_exp2f(mu * (float)((j < 4 ? tt0[j & 3] : tt1[j & 3]) >> 16)) * f0;
    SB();
    f32x4 acc0 = {0.f, 0.f, 0.f, 0.f}, acc1 = acc0;
#pragma unroll
    for (int s = 0; s < 8; s++) { acc0 = mfma_f16(fa[0][s], fb[s], acc0); acc1 = mfma_f16(fa[1][s], fb[s], acc1); }
    const float add0 = tt0[0] == 0u ? dk * osc : 0.f;
#pragma unroll
    for (int j = 0; j < 4; j++) {
      *(LAS _Float16*)(T + (tt0[j] & 0xffffu)) = (_Float16)(acc0[j] * fac[j] + (j == 0 ? add0 : 0.f));
      *(LAS _Float16*)(T + (tt1[j] & 0xffffu)) = (_Float16)(acc1[j] * fac[4 + j]);
    }
  }
  tc_poll_check(rs, 0, b, tid, p1, tag);
  tc_arrive_check(rs, 1, b, tid, ar2, tag);
  BAR();
  STAMP(6)
  u32x4 in_su[2], in_sq[2], in_xv[2], wf[48];
  f32x4 cs[3];
  const u32x2 p2 = tc_poll(rs, 1, b, tid);
#pragma unroll
  for (int j = 0; j < 2; j++) {
    const u32 t = 2 * (tid + 256 * j);
    in_su[j] = ld16(rs, t * 8, (u32)WS_STAT);
    in_sq[j] = ld16(rs, t * 8, (u32)WS_STAT + 16384);
    in_xv[j] = ld16(rs, t * 8, (u32)WS_XQ0 + (cp >> 1) * L_ * 8);
  }
  SB();
  const int grp = ((b & 1) * 8 + (b >> 5)) * 4 + w;
  const u32 o_w1 = (u32)WS_W1P + grp * 49152;
  sfor<2>([&](auto C2) {
    constexpr int c2 = C2;
    u32 wq[6];
#pragma unroll
    for (int e = 0; e < 6; e++) {
      const u32 gi = e < 4 ? gt0[e & 3] : gt1[e & 1];
      LAS char* T = sm + P_T + c2 * 1296;
      wq[e] = __builtin_bit_cast(u32, f16x2{*(LAS _Float16*)(T + (gi & 0xffffu)), *(LAS _Float16*)(T + (gi >> 16))});
    }
    const u32 a0 = __builtin_amdgcn_alignbit(wq[1], wq[0], 16), a1 = __builtin_amdgcn_alignbit(wq[2], wq[1], 16), a2 = __builtin_amdgcn_alignbit(wq[3], wq[2], 16),
              a3 = __builtin_amdgcn_alignbit(wq[4], wq[3], 16), a4 = __builtin_amdgcn_alignbit(wq[5], wq[4], 16);
    LAS char* dst = sm + M_KC + c2 * (4 * KCS) + tid * 16;
    if constexpr (c2 == 1) { tc_poll_check(rs, 1, b, tid, p2, tag); BARX(); STAMP(7) }
    sfor<4>([&](auto I) {
      constexpr int i = I;
      *(LAS u32x4*)(dst + i * KCS) = i == 0 ? u32x4{wq[0], wq[1], wq[2], wq[3]} : i == 1 ? u32x4{a0, a1, a2, a3} : i == 2 ? u32x4{wq[1], wq[2], wq[3], wq[4]} : u32x4{a1, a2, a3, a4};
      if constexpr (c2 == 1) { wf[2 * i] = ld16(rs, lane * 16 + ((2 * i) & 3) * 1024, o_w1 + (i >> 1) * 4096); wf[2 * i + 1] = ld16(rs, lane * 16 + ((2 * i + 1) & 3) * 1024, o_w1 + (i >> 1) * 4096); }
      SB();
    });
  });
  if (extra) {
    wt_proc(xt, wv2, csv);
#pragma unroll
    for (int i = 0; i < 4; i++) wt_store(xt, wv2, csv, i);
  }
  STAMP(8)
  if ((b & 1) * 8 + (b >> 5) == 15) main_body<true>(sm, ws, p.out, tag, kinv, in_su, in_sq, in_xv, wf, cs STAMP_PASS);
  else main_body<false>(sm, ws, p.out, tag, kinv, in_su, in_sq, in_xv, wf, cs STAMP_PASS);
  if (b == 0 && tid == 0) __builtin_amdgcn_raw_buffer_store_b32(tag, rs, 0, (u32)WS_EPOCH, AUX_SC1);
}

extern "C" void kernel_launch(void* const* d_in, const int* in_sizes, int n_in, void* d_out, int out_size, void* d_ws, size_t ws_size,
                              hipStream_t stream) {
  (void)hipFuncSetAttribute((const void*)k_main, hipFuncAttributeMaxDynamicSharedMemorySize, LDS_SZ(M_LDS));
  P0 p;
  p.x0 = (const u16*)d_in[0]; p.are = (const u16*)d_in[1]; p.aim = (const u16*)d_in[2]; p.bre = (const u16*)d_in[3]; p.bim = (const u16*)d_in[4];
  p.cre = (const u16*)d_in[5]; p.cim = (const u16*)d_in[6]; p.logdt = (const u16*)d_in[7]; p.dskip = (const u16*)d_in[8];
  p.wg = (const u16*)d_in[10]; p.wu = (const u16*)d_in[11]; p.wd = (const u16*)d_in[12]; p.ws = (char*)d_ws; p.out = (u16*)d_out;
  k_main<<<256, 256, LDS_SZ(M_LDS), stream>>>(p);
}
```

```cpp
#include <hip/hip_runtime.h>
#include <stdint.h>

typedef unsigned short u16;
typedef unsigned int u32;
typedef unsigned long long u64;
typedef float f32x2 __attribute__((ext_vector_type(2)));
typedef float f32x4 __attribute__((ext_vector_type(4)));
typedef double f64x2 __attribute__((ext_vector_type(2)));
typedef u32 u32x2 __attribute__((ext_vector_type(2)));
typedef u32 u32x4 __attribute__((ext_vector_type(4)));
typedef short s16x4 __attribute__((ext_vector_type(4)));
typedef __bf16 bf16x2 __attribute__((ext_vector_type(2)));
typedef __bf16 bf16x8 __attribute__((ext_vector_type(8)));
typedef _Float16 f16x2 __attribute__((ext_vector_type(2)));
typedef _Float16 f16x8 __attribute__((ext_vector_type(8)));
#define LAS __attribute__((address_space(3)))
#define DEVI __device__ __forceinline__

constexpr int L_ = 2048, D_ = 512, N_ = 128, F_ = 1376;
constexpr int NT1 = 172;
constexpr int KS2 = 43;

constexpr size_t WS_CSP = 0;
constexpr size_t WS_EPOCH = 1u << 20;
constexpr size_t WS_TAGS = WS_EPOCH + 65536;
constexpr size_t WS_STAT = (2u << 20) + 4096;
constexpr size_t WS_GRAN = WS_STAT + 2 * 32768;
constexpr size_t WS_XQ0 = WS_GRAN + 2 * 16384 + 65536;
constexpr size_t WS_XQ2 = WS_XQ0 + (2u << 20);
constexpr size_t WS_XT1 = WS_XQ2 + (2u << 20);
constexpr size_t WS_W1P = WS_XT1 + (4u << 20);
constexpr size_t WS_W2P = WS_W1P + (size_t)64 * 16 * 3 * 1024;
constexpr size_t WS_H = WS_W2P + (size_t)8 * KS2 * 4 * 1024;
constexpr size_t H_BYTES = (size_t)128 * NT1 * 256;
constexpr size_t WS_CNT = WS_H + 2 * H_BYTES;
constexpr int CNT_LOC = 6 * 256;
constexpr int CNT_LINES = CNT_LOC + 64;
constexpr size_t WS_END = WS_CNT + CNT_LINES * 128;

DEVI float bf_lo(u32 v) { return __uint_as_float(v << 16); }
DEVI float bf_hi(u32 v) { return __uint_as_float(v & 0xffff0000u); }
DEVI u32 pk_bf16(float a, float b) { f32x2 v = {a, b}; return __builtin_bit_cast(u32, __builtin_convertvector(v, bf16x2)); }
DEVI float rbf(float a) { return bf_lo(pk_bf16(a, a)); }
DEVI f32x4 mfma_bf16(u32x4 a, u32x4 b, f32x4 c) {
  return __builtin_amdgcn_mfma_f32_16x16x32_bf16(__builtin_bit_cast(bf16x8, a), __builtin_bit_cast(bf16x8, b), c, 0, 0, 0);
}
DEVI f32x4 mfma_f16(u32x4 a, u32x4 b, f32x4 c) {
  return __builtin_amdgcn_mfma_f32_16x16x32_f16(__builtin_bit_cast(f16x8, a), __builtin_bit_cast(f16x8, b), c, 0, 0, 0);
}
DEVI u32x2 tr_read(LAS char* p) { return __builtin_bit_cast(u32x2, __builtin_amdgcn_ds_read_tr16_b64_v4i16((LAS s16x4*)p)); }
DEVI float vmax(float a, float b) { float d; asm("v_max_f32 %0, %1, %2" : "=v"(d) : "v"(a), "v"(b)); return d; }
DEVI void wave_max2(float& x, float& y) {
  auto a = __builtin_amdgcn_permlane32_swap(__float_as_uint(x), __float_as_uint(y), false, false);
  float v = vmax(__uint_as_float(a[0]), __uint_as_float(a[1]));
#define DPP_MAX(ctrl) v = vmax(v, __builtin_bit_cast(float, __builtin_amdgcn_update_dpp(0, __builtin_bit_cast(int, v), ctrl, 0xf, 0xf, true)))
  DPP_MAX(0x128); DPP_MAX(0x124); DPP_MAX(0x122); DPP_MAX(0x121);
#undef DPP_MAX
  x = vmax(__builtin_bit_cast(float, __builtin_amdgcn_readlane(__builtin_bit_cast(int, v), 0)), __builtin_bit_cast(float, __builtin_amdgcn_readlane(__builtin_bit_cast(int, v), 16)));
  y = vmax(__builtin_bit_cast(float, __builtin_amdgcn_readlane(__builtin_bit_cast(int, v), 32)), __builtin_bit_cast(float, __builtin_amdgcn_readlane(__builtin_bit_cast(int, v), 48)));
}
DEVI f32x2 unp(u32 v) { return f32x2{bf_lo(v), bf_hi(v)}; }
DEVI u32 pkb(f32x2 v) { return __builtin_bit_cast(u32, __builtin_convertvector(v, bf16x2)); }
DEVI u32 pkh(f32x2 v) { return __builtin_bit_cast(u32, __builtin_convertvector(v, f16x2)); }
struct cx2 { f32x2 re, im; };
DEVI cx2 cmul(cx2 a, cx2 b) { return cx2{a.re * b.re - a.im * b.im, a.re * b.im + a.im * b.re}; }
DEVI cx2 cexp2(f32x2 zl, f32x2 zt, float m, float lg) {
  f32x2 t = zt * m, e = zl * m + lg;
  t -= f32x2{__builtin_rintf(t.x), __builtin_rintf(t.y)};
  e = f32x2{__builtin_amdgcn_exp2f(e.x), __builtin_amdgcn_exp2f(e.y)};
  return cx2{e * f32x2{__builtin_amdgcn_cosf(t.x), __builtin_amdgcn_cosf(t.y)}, e * f32x2{__builtin_amdgcn_sinf(t.x), __builtin_amdgcn_sinf(t.y)}};
}

struct P0 {
  const u16 *x0, *are, *aim, *bre, *bim, *cre, *cim, *logdt, *wg, *wu, *wd, *dskip;
  char* ws;
  u16* out;
};
constexpr int AUX_SC1 = 16;
typedef __amdgpu_buffer_rsrc_t rsrc_t;
DEVI u32x4 ld16(rsrc_t rs, u32 voff, u32 soff) { return __builtin_amdgcn_raw_buffer_load_b128(rs, voff, soff, AUX_SC1); }
DEVI void st16(rsrc_t rs, u32 voff, u32 soff, u32x4 v) { __builtin_amdgcn_raw_buffer_store_b128(v, rs, voff + soff, 0, AUX_SC1); }
DEVI void st8(rsrc_t rs, u32 voff, u32 soff, u32x2 v) { __builtin_amdgcn_raw_buffer_store_b64(v, rs, voff, soff, AUX_SC1); }
#define SB() __builtin_amdgcn_sched_barrier(0)
#define BAR() { SB(); asm volatile("s_waitcnt lgkmcnt(0)" ::: "memory"); __builtin_amdgcn_s_barrier(); asm volatile("" ::: "memory"); SB(); }
#define BARX() { SB(); asm volatile("" ::: "memory"); __builtin_amdgcn_s_barrier(); asm volatile("" ::: "memory"); SB(); }
template <int N> DEVI void wait_v() { asm volatile("s_waitcnt vmcnt(%0)" ::"n"(N) : "memory"); }
constexpr int SPIN_CAP = 1 << 18;
DEVI u32 opq(u32 x) { asm volatile("" : "+s"(x)); return x; }
template <int V> struct IC { static constexpr int v = V; constexpr operator int() const { return V; } };
template <int I, int N, class F> DEVI void sfor_(F&& f) { if constexpr (I < N) { f(IC<I>{}); sfor_<I + 1, N>(f); } }
template <int N, class F> DEVI void sfor(F&& f) { sfor_<0, N>(f); }

__device__ double buf_fadd_f64(double, __amdgpu_buffer_rsrc_t, int, int, int) __asm("llvm.amdgcn.raw.ptr.buffer.atomic.fadd.f64");
__device__ long buf_add_i64(long, __amdgpu_buffer_rsrc_t, int, int, int) __asm("llvm.amdgcn.raw.ptr.buffer.atomic.add.i64");
template <class T> DEVI void pin(T& x) { asm volatile("" : "+v"(x)); }
constexpr u32 OOB = 0x7f000000u;
template <bool Z> DEVI void mmA(f32x4& d, const u32x4& a, const u32x4& b) {
  if constexpr (Z) asm volatile("v_mfma_f32_16x16x32_bf16 %0, %1, %2, 0" : "=&v"(d) : "a"(a), "v"(b));
  else asm volatile("v_mfma_f32_16x16x32_bf16 %0, %1, %2, %0" : "+v"(d) : "a"(a), "v"(b));
}
DEVI void mmV(f32x4& d, const u32x4& a, const u32x4& b) { asm volatile("v_mfma_f32_16x16x32_bf16 %0, %1, %2, %0" : "+v"(d) : "v"(a), "v"(b)); }
DEVI void mmVz(f32x4& d, const u32x4& a, const u32x4& b) { asm volatile("v_mfma_f32_16x16x32_bf16 %0, %1, %2, 0" : "=&v"(d) : "v"(a), "v"(b)); }
template <bool Z> DEVI void mmH(f32x4& d, const u32x4& a, const u32x4& b) {
  if constexpr (Z) asm volatile("v_mfma_f32_16x16x32_f16 %0, %1, %2, 0" : "=&v"(d) : "v"(a), "v"(b));
  else asm volatile("v_mfma_f32_16x16x32_f16 %0, %1, %2, %0" : "+v"(d) : "v"(a), "v"(b));
}
#define DPP_ADD(v, ctrl) v += __builtin_bit_cast(float, __builtin_amdgcn_update_dpp(0, __builtin_bit_cast(int, v), ctrl, 0xf, 0xf, true))
#define MM_DONE() { asm volatile("s_nop 7" ::: "memory"); SB(); }

DEVI void sig_all(rsrc_t rs, int ev, int b, int tid, bool en) {
  __builtin_amdgcn_raw_ptr_buffer_atomic_add_i32(1, rs, tid < (en ? 16 : 0) ? tid * 2048 : OOB, (u32)WS_CNT + (ev * 256 + (b & 15)) * 128, 0);
  SB();
}
DEVI void sig_loc(rsrc_t rs, int ev, int tid, bool en) {
  __builtin_amdgcn_raw_ptr_buffer_atomic_add_i32(1, rs, tid < (en ? 1 : 0) ? 0 : OOB, (u32)WS_CNT + (CNT_LOC + ev) * 128, 0);
  SB();
}
DEVI u32 po_all(int ev, int b) { return (u32)WS_CNT + (ev * 256 + (b & 15) * 16) * 128; }
DEVI u32 po_loc(int ev) { return (u32)WS_CNT + (CNT_LOC + ev) * 128; }
DEVI u32 poll_issue(rsrc_t rs, u32 vo, u32 so) {
  asm volatile("" ::: "memory");
  return (u32)__builtin_amdgcn_raw_ptr_buffer_atomic_add_i32(0, rs, vo, so, 0);
}
DEVI void poll_check(rsrc_t rs, u32 vo, u32 so, u32 v, u32 need) {
  pin(v);
  if (__builtin_expect(!__all(v >= need), 0)) {
#pragma unroll 1
    for (int spin = 0; spin < SPIN_CAP; spin++) {
      if (__all(poll_issue(rs, vo, so) >= need)) break;
      __builtin_amdgcn_s_sleep(2);
    }
  }
  asm volatile("" ::: "memory");
}

DEVI u32x2 tc_add(rsrc_t rs, u32 vo, u32 so, long v) {
  asm volatile("" ::: "memory");
  return __builtin_bit_cast(u32x2, buf_add_i64(v, rs, vo, so, 0));
}
DEVI u32x2 tc_arrive(rsrc_t rs, int ev, int b, int tid) { return tc_add(rs, tid < 16 ? tid * 2048 : OOB, (u32)WS_TAGS + (ev * 256 + (b & 15)) * 128, 1); }
DEVI void tc_arrive_check(rsrc_t rs, int ev, int b, int tid, u32x2 v, u32 tag) {
  pin(v);
  bool todo = tid < 16 && v.y != tag;
  if (__builtin_expect(__any(todo), 0)) {
#pragma unroll 1
    for (int spin = 0; spin < SPIN_CAP; spin++) {
      __builtin_amdgcn_s_sleep(2);
      v = tc_add(rs, todo ? tid * 2048 : OOB, (u32)WS_TAGS + (ev * 256 + (b & 15)) * 128, 1);
      todo = todo && v.y != tag;
      if (!__any(todo)) break;
    }
  }
}
DEVI u32x2 tc_poll(rsrc_t rs, int ev, int b, int tid) { return tc_add(rs, tid < 16 ? tid * 128 : OOB, (u32)WS_TAGS + (ev * 256 + (b & 15) * 16) * 128, 0); }
DEVI void tc_poll_check(rsrc_t rs, int ev, int b, int tid, u32x2 v, u32 tag) {
  pin(v);
  if (__builtin_expect(__any(tid < 16 && (v.y != tag || v.x < 16u)), 0)) {
#pragma unroll 1
    for (int spin = 0; spin < SPIN_CAP; spin++) {
      v = tc_poll(rs, ev, b, tid);
      if (!__any(tid < 16 && (v.y != tag || v.x < 16u))) break;
      __builtin_amdgcn_s_sleep(2);
    }
  }
  asm volatile("" ::: "memory");
}

constexpr int KCS = 4128;
constexpr int M_KC = 0;
constexpr int M_W = 8 * KCS;
constexpr int M_U = M_W;
constexpr int M_XS = M_U + 2 * 4608;
constexpr int M_CRED = M_XS + 8192;
constexpr int M_X = M_W + 15 * 4096;
constexpr int M_H = M_W;
constexpr int M_RES = M_KC + 2560;
constexpr int M_LDS = M_W + 31 * 4096;

template <int LOFF, int GOFF> DEVI void gset(u32& so, u32 lbase, u32 gbase) {
  asm volatile("s_add_u32 m0, %1, %3\n\ts_add_u32 %0, %2, %4" : "=&s"(so) : "s"(lbase), "s"(gbase), "n"(LOFF), "n"(GOFF) : "memory", "scc");
}
template <int E> DEVI void gld(u32 voff, rsrc_t rs, u32 so) {
  asm volatile("buffer_load_dwordx4 %0, %1, %2 offen offset:%3 sc1 lds" ::"v"(voff), "s"(rs), "s"(so), "n"(E * 1024) : "memory");
}

template <bool STATS>
DEVI void main_body(LAS char* sm, char* __restrict__ ws, u16* __restrict__ out, const u32 GTAG, const float kinv,
                    u32x4 (&in_su)[2], u32x4 (&in_sq)[2], u32x4 (&in_xv)[2], u32x2 t2v) {
  u32x4 wf[48];
  f32x4 cs[3];
  const int w = __builtin_amdgcn_readfirstlane(threadIdx.x >> 6);
#define TIDS() int tid = threadIdx.x; asm volatile("" : "+v"(tid)); const int lane = tid & 63, r = lane & 15, g4 = lane >> 4; (void)r; (void)g4;
  const int b = blockIdx.x, xcd = b & 7, idx = b >> 3;
  const int cp = ((b >> 4) * 8 + (b & 7)) * 2 + ((b >> 3) & 1);
  const int tt = (xcd >> 1) * 4 + (idx & 3), chunk = (xcd & 1) * 8 + (idx >> 2);
  const int tb = (xcd >> 1) * 8 + (idx & 7), db = (xcd & 1) * 4 + (idx >> 3);
  const int grp = chunk * 4 + w;
  const rsrc_t rs = __builtin_amdgcn_make_buffer_rsrc((void*)ws, 0, (int)WS_END, 0x00020000);
  const rsrc_t rs_out = __builtin_amdgcn_make_buffer_rsrc((void*)out, 0, L_ * D_ * 2, 0x00020000);
  auto conv_in1 = [&](int tid, int layer, int h, auto N) {
    constexpr int j = N / 3, e = N % 3;
    const u32 vo = tid * 16;
    if constexpr (e == 0) in_su[j] = ld16(rs, vo, (u32)WS_STAT + layer * 32768 + h * 8192 + j * 4096);
    if constexpr (e == 1) in_sq[j] = ld16(rs, vo, (u32)WS_STAT + layer * 32768 + h * 8192 + 16384 + j * 4096);
    if constexpr (e == 2) in_xv[j] = ld16(rs, vo, (u32)WS_XQ0 + layer * (2u << 20) + ((cp >> 1) * L_ + 1024 * h) * 8 + j * 4096);
  };
  u32x4 A0[4][4];
#define LDW1(n) if constexpr (!STATS) wf[n] = ld16(rs, lane * 16 + ((n) & 3) * 1024, o_w1 + ((n) >> 2) * 4096)
#define OW1() const u32 o_w1 = opq((u32)WS_W1P + grp * 49152);
      auto ln = [&](int tid, int h) {
        if (h == 0 && tid < 64) *(LAS u32x4*)(sm + M_U + (tid >> 5) * 4608 + (tid & 31) * 16) = u32x4{0, 0, 0, 0};
        f32x2 mean[2], var[2], ri[2];
#pragma unroll
        for (int j = 0; j < 2; j++) {
          const f64x2 s1 = __builtin_bit_cast(f64x2, in_su[j]), s2 = __builtin_bit_cast(f64x2, in_sq[j]);
          mean[j] = f32x2{(float)s1.x, (float)s1.y} * (1.f / D_);
          var[j] = f32x2{(float)s2.x, (float)s2.y} * (1.f / D_) - mean[j] * mean[j];
        }
#pragma unroll
        for (int j = 0; j < 2; j++) var[j] = unp(pkb(f32x2{fmaxf(var[j].x, 0.f), fmaxf(var[j].y, 0.f)})) + 1e-5f;
#pragma unroll
        for (int j = 0; j < 2; j++) { var[j] = unp(pkb(var[j])); mean[j] = unp(pkb(mean[j])); }
#pragma unroll
        for (int j = 0; j < 2; j++) var[j] = f32x2{__builtin_amdgcn_sqrtf(var[j].x), __builtin_amdgcn_sqrtf(var[j].y)};
        u32 xc[2][2];
        f32x2 d[2][2];
#pragma unroll
        for (int j = 0; j < 2; j++) {
          const u32 p0 = in_xv[j].x, p1 = in_xv[j].y, p2 = in_xv[j].z, p3 = in_xv[j].w;
          const u32 xa = (cp & 1) ? p1 : p0, xb = (cp & 1) ? p3 : p2;
          xc[j][0] = __builtin_amdgcn_perm(xb, xa, 0x05040100u);
          xc[j][1] = __builtin_amdgcn_perm(xb, xa, 0x07060302u);
          d[j][0] = f32x2{bf_lo(xa), bf_lo(xb)} - mean[j];
          d[j][1] = f32x2{bf_hi(xa), bf_hi(xb)} - mean[j];
        }
#pragma unroll
        for (int j = 0; j < 2; j++) { var[j] = unp(pkb(var[j])); ri[j] = f32x2{__builtin_amdgcn_rcpf(var[j].x), __builtin_amdgcn_rcpf(var[j].y)}; }
#pragma unroll
        for (int j = 0; j < 2; j++)
#pragma unroll
          for (int cc = 0; cc < 2; cc++) {
            *(LAS u32*)(sm + M_XS + h * 4096 + cc * 2048 + (tid + 256 * j) * 4) = xc[j][cc];
            *(LAS u32*)(sm + M_U + cc * 4608 + 512 + 2048 * h + (tid + 256 * j) * 4) = pkh(d[j][cc] * ri[j]);
          }
      };
  {
    TIDS()
    OW1()
    ln(tid, 0);
    tc_poll_check(rs, 1, b, tid, t2v, GTAG);
    BAR();
    const int cpy = (3 - r) & 3;
    LAS char* pa = sm + M_KC + (w >> 1) * (4 * KCS) + cpy * KCS + 2 * (15 + 8 * g4 - r - cpy) + 64 * (4 - 4 * (w & 1)) + 2048;
    sfor<16>([&](auto n) {
      constexpr int rr = n / 4, i = n % 4;
      const u32x2 lo = *(LAS u32x2*)(pa + 512 * (3 - i) + 64 * (3 - rr)), hi = *(LAS u32x2*)(pa + 512 * (3 - i) + 64 * (3 - rr) + 8);
      A0[rr][i] = u32x4{lo.x, lo.y, hi.x, hi.y};
      if constexpr (n < 15) { LDW1(n); }
      if constexpr (!STATS && n % 2 == 1 && n < 6) cs[n / 2] = __builtin_bit_cast(f32x4, ld16(rs, r * 256 + g4 * 16, (u32)WS_CSP + ((grp * 3 + n / 2) >> 2) * 4096 + ((grp * 3 + n / 2) & 3) * 64));
      SB();
    });
  }
#pragma unroll 1
  for (int layer = 0; layer < 2; layer++) {
    const u32 o_xt = opq((u32)WS_XT1 + layer * (2u << 20));
    const u32 o_h = opq((u32)WS_H + layer * (u32)H_BYTES);
    const u32 o_gr = (u32)WS_GRAN + layer * 16384;
    u32x4 A2[43];
    const u32 o_w2 = opq((u32)WS_W2P + (db * 4 + w) * KS2 * 1024);
    auto ldw2 = [&](int lane, auto n) { A2[n] = ld16(rs, lane * 16 + (n & 3) * 1024, o_w2 + (n >> 2) * 4096); };
#define LDWC(n) if constexpr (STATS) ldw2(lane, IC<(n) - 15>{}); else wf[n] = ld16(rs, lane * 16 + ((n) & 3) * 1024, o_w1 + ((n) >> 2) * 4096)
    u32 gv0, gso;
    const u32 ldsw = opq((u32)(uintptr_t)sm + w * 4096);
    const u32 o_xs = opq(o_xt + tt * 65536 + w * 4096);
    auto stage = [&](auto H, auto N) {
      if constexpr (N % 4 == 0) gset<M_X + (N / 4) * 16384, H * (1 << 20) + (N / 4) * 16384>(gso, ldsw, o_xs);
      gld<N % 4>(gv0, rs, gso);
    };
    {
      TIDS()
      OW1()
      gv0 = lane * 16;
      const int c = w >> 1, half = w & 1;
      const int cpy = (3 - r) & 3;
      LAS char* pa = sm + M_KC + c * (4 * KCS) + cpy * KCS + 2 * (15 + 8 * g4 - r - cpy) + 64 * (4 - 4 * half);
      LAS char* pb = sm + M_U + c * 4608 + 2 * (240 + 16 * r + 8 * g4) - 64 * (4 * half + 3);
      u32x4 A1[4][4], B[4][4];
      f32x4 acc[4], accH[4];
      auto rdA = [&](int rr, int i) {
        const u32x2 lo = *(LAS u32x2*)(pa + 512 * (3 - i) + 64 * (3 - rr)), hi = *(LAS u32x2*)(pa + 512 * (3 - i) + 64 * (3 - rr) + 8);
        A1[rr][i] = u32x4{lo.x, lo.y, hi.x, hi.y};
      };
      LAS char* cred = sm + M_CRED + w * 4096 + lane * 16;
      auto tri = [&](auto H, f32x4 (&ac)[4], auto side) {
        constexpr int h = H;
        sfor<16>([&](auto n) { B[n / 4][n % 4] = *(LAS u32x4*)(pb + 2048 * h + 512 * (n % 4) + 64 * (3 - n / 4)); SB(); });
        sfor<40>([&](auto n) {
          constexpr int g = n < 16 ? 3 : n < 28 ? 2 : n < 36 ? 1 : 0, e = n - (g == 3 ? 0 : g == 2 ? 16 : g == 1 ? 28 : 36);
          constexpr int rr = e / (g + 1), m = e % (g + 1);
          mmH<(h == 0 && e == 0)>(ac[g], A0[rr][g - m], B[rr][m]);
          if constexpr (n == 24) *(LAS f32x4*)(cred + 3072) = ac[3];
          if constexpr (n == 36) *(LAS f32x4*)(cred + 2048) = ac[2];
          side(n);
          SB();
        });
      };
      f32x4 kk[2], rv[2];
      u32x2 xr[2];
      u32 o[2][2];
      auto red_wr = [&](f32x4 (&ac)[4], int g) { *(LAS f32x4*)(cred + g * 1024) = ac[g]; };
      auto epi_rd = [&](int h, int g) {
        LAS char* p0 = sm + M_CRED + (w & 2) * 4096 + half * 2048 + lane * 16 + g * 1024;
        kk[g] = *(LAS f32x4*)p0;
        rv[g] = *(LAS f32x4*)(p0 + 4096);
        xr[g] = *(LAS u32x2*)(sm + M_XS + h * 4096 + c * 2048 + (256 * (2 * half + g) + 16 * r + 4 * g4) * 2);
      };
      auto epi_c = [&](int g, int e) {
        const f32x2 v = e ? f32x2{kk[g].z, kk[g].w} + f32x2{rv[g].z, rv[g].w} : f32x2{kk[g].x, kk[g].y} + f32x2{rv[g].x, rv[g].y};
        o[g][e] = pkb(v * kinv + unp(e ? xr[g].y : xr[g].x));
      };
      const int dch = cp * 2 + c, fd = ((dch >> 1) & 1) | (((dch >> 3) & 1) << 1);
      const u32 vo_st = (4 * (2 * half + (g4 & 1)) + (r >> 2)) * 65536 + ((r & 3) ^ fd) * 32 + (g4 & 2) * 8 + o_xt + dch * 128;
      auto epi_st = [&](int h) {
        auto e0 = __builtin_amdgcn_permlane16_swap(o[0][0], o[1][0], false, false);
        auto e1 = __builtin_amdgcn_permlane16_swap(o[0][1], o[1][1], false, false);
        st16(rs, vo_st, h << 20, u32x4{e0[0], e1[0], e0[1], e1[1]});
      };
      const u32 pvo = tid < 16 ? tid * 128 : OOB;
      const u32 n32 = tid < 16 ? 16 : 0;
      u32 pv;
      { u32 a_ = (u32)(uintptr_t)pa, b_ = (u32)(uintptr_t)pb; pin(a_); pin(b_); pa = (LAS char*)(uintptr_t)a_; pb = (LAS char*)(uintptr_t)b_; pin(gv0); }
      BAR();
      tri(IC<0>{}, acc, [&](auto n) {
        if constexpr (n % 8 == 3) { LDWC(15 + n / 8); }
        if constexpr (n >= 24 && n % 4 == 1) rdA(0, (n - 24) / 4);
        if constexpr (n == 14) { wait_v<(14 + 5) / 8>(); BARX(); sig_all(rs, 3, b, tid, layer); }
      });
      sfor<64>([&](auto n) {
        constexpr int rr = n / 16, m = 3 - (n % 16) / 4, g = n % 4, s = n / 4, ia = 4 + g - m;
        mmH<(n < 4)>(accH[g], ia < 4 ? A0[rr][ia & 3] : A1[rr][ia & 3], B[rr][m]);
        if constexpr (n == 3) red_wr(acc, 1);
        if constexpr (n == 6) red_wr(acc, 0);
        if constexpr (n == 10) { BAR(); epi_rd(0, 0); epi_rd(0, 1); }
        if constexpr (n >= 15 && n < 23 && n % 2 == 1) epi_c((n - 15) / 4, ((n - 15) / 2) & 1);
        if constexpr (n == 23) epi_st(0);
        if constexpr (n % 4 == 0 && rr < 3 && n > 0) rdA((n - 4) / 16 + 1, (s - 1) % 4);
        if constexpr (n == 48) rdA(3, 3);
        if constexpr (n >= 24 && n % 5 == 1 && n < 46) { LDWC(20 + (n - 26) / 5); }
        if constexpr (n == 2) pv = poll_issue(rs, pvo, po_all(3, b));
        if constexpr (n == 46) { poll_check(rs, pvo, po_all(3, b), pv, layer ? n32 : 0); BARX(); }
        if constexpr (n > 46 && (n - 46) % 2 == 1 && (n - 46) / 2 < 6) conv_in1(tid, layer, 1, IC<(n - 46) / 2>{});
        SB();
      });
      static_assert(46 + 11 < 64 && 46 >= 42 && 2 < 23, "");
      sfor<4>([&](auto n) { LDWC(24 + n); });
      wait_v<14>();
      BARX();
      sig_all(rs, layer * 4, b, tid, true);
      sfor<4>([&](auto n) { LDWC(28 + n); });
      SB();
      ln(tid, 1);
      BAR();
      tri(IC<1>{}, accH, [&](auto n) {
        if constexpr (n % 6 == 3 && n < 24) { LDWC(32 + n / 6); }
        if constexpr (n == 4) pv = poll_issue(rs, pvo, po_all(layer * 4, b));
      });
      asm volatile("s_nop 3");
      red_wr(accH, 1);
      asm volatile("s_nop 3");
      SB();
      red_wr(accH, 0);
      poll_check(rs, pvo, po_all(layer * 4, b), pv, n32);
      BAR();
      sfor<2>([&](auto k) { stage(IC<0>{}, k); });
      epi_rd(1, 0); epi_rd(1, 1);
      sfor<2>([&](auto k) { stage(IC<0>{}, IC<2 + k>{}); });
      SB();
      epi_c(0, 0); epi_c(0, 1); epi_c(1, 0); epi_c(1, 1);
      epi_st(1);
      SB();
      sfor<4>([&](auto k) { stage(IC<0>{}, IC<4 + k>{}); });
    }
    u32x4 res;
    f32x4 ac2[4];
    u32x2 rx[2];
    auto res_a = [&](int lane) {
      LAS char* rb = sm + M_RES + w * KCS;
      *(LAS u32x4*)(rb + (lane >> 2) * 80 + (lane & 3) * 16) = res;
#pragma unroll
      for (int i = 0; i < 2; i++) rx[i] = tr_read(rb + (lane >> 2) * 80 + i * 32 + (lane & 3) * 8);
    };
    auto res_b = [&]() {
#pragma unroll
      for (int i = 0; i < 2; i++) { ac2[i] = f32x4{bf_lo(rx[i].x), bf_hi(rx[i].x), bf_lo(rx[i].y), bf_hi(rx[i].y)}; pin(ac2[i]); }
      SB();
    };
    const u32 o_hr = opq(o_h + (tb * 2 + (w & 1)) * (NT1 * 256) + (w >> 1) * 4096);
    auto sth = [&](auto H, auto N) {
      if constexpr (N % 4 == 0) gset<M_H + (N / 4) * 16384, H * 64 * (NT1 * 256) + (N / 4) * 8192>(gso, ldsw, o_hr);
      gld<N % 4>(gv0, rs, gso);
    };
    auto ldres = [&](int tid, int h) {
      const int dl = tid >> 2, fd = ((dl >> 1) & 1) | (((dl >> 3) & 1) << 1);
      res = ld16(rs, dl * 128 + ((2 * (tb & 1) + ((tid >> 1) & 1)) ^ fd) * 32 + (tid & 1) * 16, o_xt + (h << 20) + (tb >> 1) * 65536 + db * 8192);
    };
    {
      TIDS()
      const int q = r >> 2, pp = lane & 3;
      const int fsw = ((q >> 1) & 1) | ((g4 & 1) << 1);
      const u32 pvo = tid < 16 ? tid * 128 : OOB, pv0 = tid == 0 ? 0 : OOB, n32 = tid < 16 ? 16 : 0, n15 = tid == 0 ? 15 : 0;
      const u32 pah = po_all(layer * 4 + 1, b), pbl = po_loc((layer * 2) * 16 + (tb >> 1));
      if constexpr (STATS) {
        sfor<8>([&](auto k) { stage(IC<0>{}, IC<8 + k>{}); });
        LAS char* xr = sm + M_X + (8 * g4 + q) * 128 + pp * 8 + 32 * (w ^ fsw);
        u32 pv;
        sfor<2>([&](auto H) {
          constexpr int h = H;
          const u32x4 ones = {0x3f803f80u, 0x3f803f80u, 0x3f803f80u, 0x3f803f80u};
          f32x4 as = {0.f, 0.f, 0.f, 0.f}, aq = as;
          sfor<2>([&](auto KK) {
            wait_v<KK == 0 ? 8 : h == 0 ? 1 : 0>();
            BARX();
            if constexpr (h == 0 && KK == 0) sig_all(rs, layer * 4 + 1, b, tid, true);
            u32x2 lo[8], hi[8];
#pragma unroll
            for (int k = 0; k < 8; k++) { lo[k] = tr_read(xr + (8 * KK + k) * 4096); hi[k] = tr_read(xr + (8 * KK + k) * 4096 + 512); }
            SB();
#pragma unroll
            for (int k = 0; k < 8; k++) {
              const u32x4 fr = {lo[k].x, lo[k].y, hi[k].x, hi[k].y};
              as = mfma_bf16(ones, fr, as);
              aq = mfma_bf16(fr, fr, aq);
            }
            pin(as); pin(aq);
          });
          const float ssq = pp == 0 ? aq[0] : pp == 1 ? aq[1] : pp == 2 ? aq[2] : aq[3];
          const float mean = as[0] * (1.f / D_);
          const float var = fmaxf(ssq * (1.f / D_) - mean * mean, 0.f);
          const float sd = __builtin_amdgcn_sqrtf(rbf(rbf(var) + 1e-5f));
          st8(rs, q == g4 ? (w * 16 + r) * 8 : OOB, o_gr + (1024 * h + tt * 64) * 8, u32x2{pk_bf16(mean, sd), GTAG});
          SB();
          if constexpr (h == 0) {
            sfor<10>([&](auto n) { ldw2(lane, IC<21 + n>{}); });
            SB();
            pv = poll_issue(rs, pvo, pah);
            poll_check(rs, pvo, pah, pv, n32);
            BAR();
            sfor<16>([&](auto k) { stage(IC<1>{}, k); });
          }
        });
        sfor<7>([&](auto n) { ldw2(lane, IC<31 + n>{}); });
        ldres(tid, 0);
        pv = poll_issue(rs, pv0, pbl);
        poll_check(rs, pv0, pbl, pv, n15);
        res_a(lane);
        BAR();
        sfor<23>([&](auto j) { sth(IC<0>{}, j); });
      } else {
        f32x4 acc[4][3];
        u32x2 gr[4];
        u32 hvo[3];
#pragma unroll
        for (int nn = 0; nn < 3; nn++) {
          const int item = 4 * nn + g4, ii = item / 3, jj = item - 3 * ii;
          hvo[nn] = ii * (NT1 * 256) + jj * 256 + r * 16 + ((grp * 3 + jj < NT1) ? 0u : OOB) + o_h + tt * 4 * (NT1 * 256) + grp * 768;
          pin(hvo[nn]);
        }
        u32 pv;
        auto kloop = [&](auto H, auto side) {
          constexpr int h = H;
          LAS char* xr = sm + M_X + (8 * g4 + q) * 128 + pp * 8;
          u32x2 lo[2][4], hi[2][4];
          sfor<4>([&](auto i) { lo[0][i] = tr_read(xr + 32 * (i ^ fsw)); hi[0][i] = tr_read(xr + 32 * (i ^ fsw) + 512); SB(); });
          sfor<64>([&](auto n) {
            constexpr int k = n / 4, i = n % 4;
            const u32x4 xf = {lo[k & 1][i].x, lo[k & 1][i].y, hi[k & 1][i].x, hi[k & 1][i].y};
            side(IC<2 * n>{});
            SB();
            mmA<k == 0>(acc[i][0], wf[k * 3], xf);
            if constexpr (k < 15) lo[(k + 1) & 1][i] = tr_read(xr + (k + 1) * 4096 + 32 * (i ^ fsw));
            SB();
            mmA<k == 0>(acc[i][1], wf[k * 3 + 1], xf);
            if constexpr (k < 15) hi[(k + 1) & 1][i] = tr_read(xr + (k + 1) * 4096 + 32 * (i ^ fsw) + 512);
            SB();
            mmA<k == 0>(acc[i][2], wf[k * 3 + 2], xf);
            side(IC<2 * n + 1>{});
            SB();
          });
          MM_DONE();
        };
        auto ldgr = [&](int h, int i) { gr[i] = __builtin_amdgcn_raw_buffer_load_b64(rs, r * 8 + i * 128, o_gr + (1024 * h + tt * 64) * 8, AUX_SC1); };
        auto epi1 = [&](int h, auto side) {
#define GR_OK() __all((gr[0].y == GTAG) & (gr[1].y == GTAG) & (gr[2].y == GTAG) & (gr[3].y == GTAG))
          if (__builtin_expect(!GR_OK(), 0)) {
#pragma unroll 1
            for (int spin = 0; spin < SPIN_CAP; spin++) {
              asm volatile("" ::: "memory");
              ldgr(h, 0); ldgr(h, 1); ldgr(h, 2); ldgr(h, 3);
              if (GR_OK()) break;
            }
          }
          u32 hv[12];
          sfor<4>([&](auto I) {
            constexpr int i = I;
            const float m = bf_lo(gr[i].x), sd = bf_hi(gr[i].x);
            const float kx = -1.44269504088896341f * __builtin_amdgcn_rcpf(sd), cd = -1.44269504088896341f * sd;
            f32x2 y[3], au[3], e[3];
#pragma unroll
            for (int j = 0; j < 3; j++) y[j] = f32x2{acc[i][j].x, acc[i][j].y} - m * f32x2{cs[j].x, cs[j].y};
#pragma unroll
            for (int j = 0; j < 3; j++) { y[j] *= kx; au[j] = f32x2{acc[i][j].z, acc[i][j].w} - m * f32x2{cs[j].z, cs[j].w}; }
#pragma unroll
            for (int j = 0; j < 3; j++) e[j] = f32x2{__builtin_amdgcn_exp2f(y[j].x), __builtin_amdgcn_exp2f(y[j].y)};
            side(IC<3 * i>{});
#pragma unroll
            for (int j = 0; j < 3; j++) { e[j] = e[j] * cd + cd; y[j] *= au[j]; }
            side(IC<3 * i + 1>{});
#pragma unroll
            for (int j = 0; j < 3; j++) e[j] = f32x2{__builtin_amdgcn_rcpf(e[j].x), __builtin_amdgcn_rcpf(e[j].y)};
            side(IC<3 * i + 2>{});
#pragma unroll
            for (int j = 0; j < 3; j++) hv[3 * i + j] = pkb(y[j] * e[j]);
            if constexpr (i >= 1) {
              constexpr int nn = i - 1;
              auto s02 = __builtin_amdgcn_permlane32_swap(hv[4 * nn], hv[4 * nn + 2], false, false);
              auto s13 = __builtin_amdgcn_permlane32_swap(hv[4 * nn + 1], hv[4 * nn + 3], false, false);
              auto a01 = __builtin_amdgcn_permlane16_swap(s02[0], s13[0], false, false);
              auto a23 = __builtin_amdgcn_permlane16_swap(s02[1], s13[1], false, false);
              st16(rs, hvo[nn], h * 64 * (NT1 * 256), u32x4{a01[0], a01[1], a23[0], a23[1]});
            }
            SB();
          });
        };
        OW1()
        if (layer == 0) {
#pragma unroll
          for (int j = 0; j < 3; j++)
#pragma unroll
            for (int e = 0; e < 4; e++) { float v = cs[j][e]; DPP_ADD(v, 0x128); DPP_ADD(v, 0x124); DPP_ADD(v, 0x122); DPP_ADD(v, 0x121); cs[j][e] = v; }
        }
        wait_v<5>();
        BARX();
        kloop(IC<0>{}, [&](auto n2) {
          constexpr int k = n2 / 8, i = (n2 / 2) % 4, post = n2 & 1;
          if constexpr (!post && i == 0 && k == 3) { wait_v<9>(); BARX(); sig_all(rs, layer * 4 + 1, b, tid, true); }
          if constexpr (!post && i == 0 && k == 7) { wait_v<10>(); BARX(); }
          if constexpr (!post && i == 0 && k == 11) { wait_v<7>(); BARX(); }
          if constexpr (post && i == 0 && k < 8) stage(IC<0>{}, IC<8 + k>{});
          if constexpr (post && (i & 1) && k < 6) { LDW1(36 + 2 * k + (i >> 1)); }
          if constexpr (post && i == 2 && k >= 6 && k < 14) ldw2(lane, IC<(k < 8 ? k - 6 : 2 * k - 14)>{});
          if constexpr (post && i == 0 && k >= 8 && k < 14) ldw2(lane, IC<2 * k - 13>{});
          if constexpr (post && k == 13 && (i & 1)) { ldgr(0, 2 * (i >> 1)); ldgr(0, 2 * (i >> 1) + 1); }
          if constexpr (post && i == 1 && k == 11) pv = poll_issue(rs, pvo, pah);
        });
        poll_check(rs, pvo, pah, pv, n32);
        BARX();
        epi1(0, [&](auto n) {
          if constexpr (n < 4) { stage(IC<1>{}, IC<2 * n>{}); stage(IC<1>{}, IC<2 * n + 1>{}); }
          else stage(IC<1>{}, IC<4 + n>{});
        });
        wait_v<15>();
        BAR();
        kloop(IC<1>{}, [&](auto n2) {
          constexpr int k = n2 / 8, i = (n2 / 2) % 4, post = n2 & 1;
          if constexpr (!post && i == 0 && k == 3) { wait_v<9 + 4>(); BARX(); }
          if constexpr (!post && i == 0 && k == 5) { wait_v<8>(); BARX(); sig_loc(rs, (layer * 2) * 16 + tt, tid, true); }
          if constexpr (post && !(i & 1) && k >= 1 && k < 13) ldw2(lane, IC<12 + 2 * k + (i >> 1)>{});
          if constexpr (post && i == 1 && k == 10) pv = poll_issue(rs, pv0, pbl);
          if constexpr (post && k == 10 && (i & 1)) { ldgr(1, 2 * (i >> 1)); ldgr(1, 2 * (i >> 1) + 1); }
          if constexpr (post && i == 3 && k == 9) ldres(tid, 0);
          if constexpr (!post && i == 0 && k == 14) { pin(gr[3]); poll_check(rs, pv0, pbl, pv, n15); BARX(); }
          if constexpr (post && i < 3 && k >= 14) sth(IC<0>{}, IC<3 * (k - 14) + i>{});
          if constexpr (post && i == 3 && k == 15) res_a(lane);
        });
        BARX();
        epi1(1, [&](auto n) {
          if constexpr (n < 5) { sth(IC<0>{}, IC<6 + 2 * n>{}); sth(IC<0>{}, IC<7 + 2 * n>{}); }
          else sth(IC<0>{}, IC<11 + n>{});
        });
      }
    }
    {
      TIDS()
      OW1()
      auto mm2 = [&](auto side) {
        constexpr int PD = 8;
        u32x4 Bf[10];
        LAS char* hb = sm + M_H + lane * 16;
        LAS char* hb2 = hb + 32768;
        auto rdh = [&](auto n) {
          constexpr int s = n / 2, o = (s >> 3) * 16384 + (((s >> 2) & 1) * 2 + (n & 1)) * 4096 + (s & 3) * 1024;
          Bf[n % 10] = o < 32768 ? *(LAS u32x4*)(hb + o) : *(LAS u32x4*)(hb2 + (o - 32768));
        };
        sfor<PD>([&](auto n) { rdh(n); SB(); });
        sfor<86>([&](auto n) {
          constexpr int m = n ^ 1;
          side(IC<2 * n>{});
          SB();
          if constexpr (m == 2 || m == 3) mmVz(ac2[m], A2[1], Bf[m]);
          else if constexpr (m / 2 < 10) mmV(ac2[m & 3], A2[m / 2], Bf[m % 10]);
          else mmA<false>(ac2[m & 3], A2[m / 2], Bf[m % 10]);
          if constexpr (n + PD < 86) rdh(IC<n + PD>{});
          side(IC<2 * n + 1>{});
          SB();
        });
        MM_DONE();
      };
      const u32 ovo = r * (layer ? D_ * 2 : 8) + g4 * (layer ? 8 : L_ * 8);
      auto epi2 = [&](int h) {
        float s1[2], s2[2];
#pragma unroll
        for (int i = 0; i < 2; i++) {
          const f32x4 v = ac2[i] + ac2[2 + i];
          const u32 o0 = pkb(f32x2{v.x, v.y}), o1 = pkb(f32x2{v.z, v.w});
          st8(rs, layer ? OOB : ovo, (u32)WS_XQ2 + ((db * 16 + 4 * w) * L_ + 1024 * h + tb * 32 + 16 * i) * 8, u32x2{o0, o1});
          __builtin_amdgcn_raw_buffer_store_b64(u32x2{o0, o1}, rs_out, layer ? ovo : OOB, ((1024 * h + tb * 32 + 16 * i) * D_ + db * 64 + 16 * w) * 2, 0);
          const float a = bf_lo(o0), bq = bf_hi(o0), cq = bf_lo(o1), dq = bf_hi(o1);
          s1[i] = (a + bq) + (cq + dq);
          s2[i] = (a * a + bq * bq) + (cq * cq + dq * dq);
        }
        auto x0 = __builtin_amdgcn_permlane16_swap(__float_as_uint(s1[0]), __float_as_uint(s2[0]), false, false);
        auto x1 = __builtin_amdgcn_permlane16_swap(__float_as_uint(s1[1]), __float_as_uint(s2[1]), false, false);
        const float t0 = __uint_as_float(x0[0]) + __uint_as_float(x0[1]), t1 = __uint_as_float(x1[0]) + __uint_as_float(x1[1]);
        auto x2 = __builtin_amdgcn_permlane32_swap(__float_as_uint(t0), __float_as_uint(t1), false, false);
        const float t2 = __uint_as_float(x2[0]) + __uint_as_float(x2[1]);
        buf_fadd_f64((double)t2, rs, layer ? OOB : (g4 & 1) * 16384 + ((g4 >> 1) * 16 + r) * 8, (u32)WS_STAT + 32768 + (1024 * h + tb * 32) * 8, 0);
        SB();
      };
      const u32 pbh = po_loc((layer * 2 + 1) * 16 + (tb >> 1)), pvo = tid < 16 ? tid * 128 : OOB, pv0 = tid == 0 ? 0 : OOB;
      u32 pv;
      res_b();
      wait_v<STATS ? 15 : 18>();
      BARX();
      mm2([&](auto n2) {
        constexpr int n = n2 / 2, post = n2 & 1, j = n / 4;
        if constexpr (post && n % 4 == 1 && j < 5) ldw2(lane, IC<38 + j>{});
        if constexpr (post && n % 4 == 1 && j == 5) ldres(tid, 1);
        if constexpr (post && n % 4 == 1 && j >= 6 && j < 21) { LDW1(j - 6); }
        if constexpr (!post && n == 24) { wait_v<STATS ? 7 + 6 : 10 + 6>(); BARX(); }
        if constexpr (!post && n == 48) { wait_v<STATS ? 6 : 48 / 4>(); BARX(); if constexpr (!STATS) sig_loc(rs, (layer * 2 + 1) * 16 + tt, tid, true); }
        if constexpr (post && n == 85) pv = poll_issue(rs, pv0, pbh);
        if constexpr (post && n == 79) res_a(lane);
      });
      epi2(0);
      res_b();
      poll_check(rs, pv0, pbh, pv, tid == 0 ? 15 : 0);
      BARX();
      sfor<12>([&](auto j) { sth(IC<1>{}, j); });
      wait_v<8>();
      BARX();
      sig_all(rs, 2, b, tid, !layer);
      mm2([&](auto n2) {
        constexpr int n = n2 / 2, post = n2 & 1, j = n / 4;
        if constexpr (post && n % 4 == 1 && j + 12 < 23) sth(IC<1>{}, IC<12 + j>{});
        if constexpr (!post && n % 16 == 8 && n < 80) {
          constexpr int J = (n + 8) / 16, issued = 12 + j < 23 ? 12 + j : 23, need = J == 4 ? 23 : 4 * J + 4;
          if constexpr (J < 5) wait_v<issued - need>();
          if constexpr (J == 5) poll_check(rs, pvo, po_all(2, b), pv, (tid < 16 && !layer) ? 16 : 0);
          BARX();
        }
        if constexpr (post && n == 34) pv = poll_issue(rs, layer ? OOB : pvo, po_all(2, b));
        if constexpr (post && n >= 73 && n % 2 == 1 && n < 85) conv_in1(tid, 1, 0, IC<(n - 73) / 2>{});
      });
      epi2(1);
      if (layer) break;
      ln(tid, 0);
      SB();
    }
  }
}

constexpr int P_WT = M_W;
constexpr int P_VT = P_WT + 2 * 32 * 528;
constexpr int P_T = P_VT + 2 * 32 * 528;
constexpr int P_TR = P_T + 2 * 648 * 2;
static_assert(P_TR + 4 * 32 * 144 <= M_LDS && P_TR % 16 == 0, "");

constexpr int c_rne(int l) {
  int sh = 0;
  while ((l >> sh) >= 256) sh++;
  if (sh == 0) return l;
  const int q = l >> sh, rem = l & ((1 << sh) - 1), half = 1 << (sh - 1);
  return (q + ((rem > half || (rem == half && (q & 1))) ? 1 : 0)) << sh;
}
constexpr int c_idx(int lam) { int sh = 0; while ((lam >> sh) >= 256) sh++; return sh == 0 ? lam : 128 * sh + (lam >> sh); }
struct Tabs { u16 g[256][16]; u32 t[128][8]; };
constexpr Tabs make_tabs() {
  Tabs x{};
  for (int t = 0; t < 256; t++)
    for (int e = 0; e < 16; e++) { const int l = 2047 - (8 * t + e); x.g[t][e] = (u16)(2 * (l < 0 ? 645 : c_idx(c_rne(l)))); }
  for (int i = 0; i < 128; i++)
    for (int j = 0; j < 8; j++) {
      const int col = 16 * (i >> 6) + (i & 15), row = 16 * (j >> 2) + 4 * ((i >> 4) & 3) + (j & 3);
      const int al = row < 8 ? row : row < 12 ? 256 + 2 * (row - 8) : row < 16 ? 512 + 256 * ((row - 12) >> 1) + 4 * (row & 1) : 1024 + 256 * (row - 16);
      const int lam = al + 8 * col;
      const bool ok = row < 20 || (row == 20 && col == 0);
      x.t[i][j] = ok ? ((u32)lam << 16) | (u32)(2 * c_idx(lam)) : (u32)(2 * 644) | (1u << 16);
    }
  return x;
}
__device__ const Tabs g_tabs __attribute__((aligned(128))) = make_tabs();

__global__ void __launch_bounds__(256, 1) k_main(P0 p) {
  extern __shared__ __attribute__((aligned(16))) char smem_[];
  LAS char* sm = (LAS char*)smem_;
  char* ws = p.ws;
  const int b = blockIdx.x, tid = threadIdx.x, lane = tid & 63, w = __builtin_amdgcn_readfirstlane(tid >> 6);
  const int r = lane & 15, g4 = lane >> 4;
  const rsrc_t rs = __builtin_amdgcn_make_buffer_rsrc((void*)ws, 0, (int)WS_END, 0x00020000);
  constexpr int CODE_BYTES = 46080;
  const u32 code_pf = __builtin_amdgcn_raw_buffer_load_b32(
      __builtin_amdgcn_make_buffer_rsrc((void*)(__builtin_amdgcn_s_getpc() & ~127ull), 0, CODE_BYTES, 0x00020000),
      tid < 12 ? ((b >> 3) * 12 + tid) * 128 : OOB, 0, 0);
  SB();
  auto wt_load = [&](int task, u32x4 (&v)[4]) {
    const bool up = task < 688;
    const int t2 = task - 688;
    const u32 so = up ? ((task & 15) * 32 * F_ + (task >> 4) * 32) * 2 : ((t2 >> 3) * 32 * D_ + (t2 & 7) * 64) * 2;
    const u32 ld = up ? F_ * 2 : D_ * 2;
    const rsrc_t r0 = __builtin_amdgcn_make_buffer_rsrc((void*)(up ? p.wg : p.wd), 0, D_ * F_ * 2, 0x00020000);
    const rsrc_t r1 = __builtin_amdgcn_make_buffer_rsrc((void*)(up ? p.wu : p.wd), 0, D_ * F_ * 2, 0x00020000);
    const u32 vo = (lane >> 2) * ld + (lane & 3) * 16, s1 = so + (up ? 0 : 64);
    v[0] = __builtin_amdgcn_raw_buffer_load_b128(r0, vo, so, 0); v[1] = __builtin_amdgcn_raw_buffer_load_b128(r1, vo, s1, 0);
    v[2] = __builtin_amdgcn_raw_buffer_load_b128(r0, vo, so + 16 * ld, 0); v[3] = __builtin_amdgcn_raw_buffer_load_b128(r1, vo, s1 + 16 * ld, 0);
  };
  auto wt_proc = [&](int task, u32x4 (&v)[4], u32x4& csv) {
    LAS char* tr = sm + P_TR + w * (32 * 144);
    if (task < 688) {
#pragma unroll
      for (int i = 0; i < 2; i++) {
        LAS char* dst = tr + (i * 16 + (lane >> 2)) * 144 + (lane & 3) * 32;
        *(LAS u32x4*)dst = u32x4{v[2 * i].x, v[2 * i + 1].x, v[2 * i].y, v[2 * i + 1].y};
        *(LAS u32x4*)(dst + 16) = u32x4{v[2 * i].z, v[2 * i + 1].z, v[2 * i].w, v[2 * i + 1].w};
      }
    } else {
#pragma unroll
      for (int i = 0; i < 4; i++) *(LAS u32x4*)(tr + ((i >> 1) * 16 + (lane >> 2)) * 144 + (i & 1) * 64 + (lane & 3) * 16) = v[i];
    }
    f32x4 cs = {0.f, 0.f, 0.f, 0.f};
#pragma unroll
    for (int i = 0; i < 4; i++) {
      LAS char* a = tr + (8 * g4 + (r >> 2)) * 144 + i * 32 + (lane & 3) * 8;
      const u32x2 lo = tr_read(a), hi = tr_read(a + 4 * 144);
      v[i] = u32x4{lo.x, lo.y, hi.x, hi.y};
      const u32 one = r == i ? 0x3f803f80u : 0u;
      cs = mfma_bf16(v[i], u32x4{one, one, one, one}, cs);
    }
    csv = __builtin_bit_cast(u32x4, cs);
  };
  auto wt_store = [&](int task, u32x4 (&v)[4], const u32x4& csv, int i) {
    const bool up = task < 688;
    if (i == 4) {
      st16(rs, (r < 4 && up) ? r * 64 + g4 * 16 : OOB, (u32)WS_CSP + task * 256, csv);
    } else {
      const int t2 = task - 688, tw = (task >> 4) * 4 + i, g3 = (tw * 43691) >> 17;
      const u32 o1 = (u32)WS_W1P + (g3 * 45 + (task & 15) * 3 + tw) * 1024, o2 = (u32)WS_W2P + (((t2 & 7) * 4 + i) * KS2 + (t2 >> 3)) * 1024;
      st16(rs, lane * 16, up ? o1 : o2, v[i]);
    }
    SB();
  };
  const int gw0 = b * 4 + w, ksq = (gw0 * 1525) >> 16;
  const int gw = gw0 < 688 ? (gw0 - ksq * 43) * 16 + ksq : gw0;
  u32x4 wv[4], xin[2], xs[2], csv;
  const u32 epoch = __builtin_amdgcn_raw_buffer_load_b32(rs, 0, (u32)WS_EPOCH, AUX_SC1);
  {
    const rsrc_t rx = __builtin_amdgcn_make_buffer_rsrc((void*)p.x0, 0, L_ * D_ * 2, 0x00020000);
#pragma unroll
    for (int i = 0; i < 2; i++) {
      xin[i] = __builtin_amdgcn_raw_buffer_load_b128(rx, (tid & 7) * 2048 + (tid >> 3) * 16 + i * 1024, (b >> 1) * 16384 + (b & 1) * 512, 0);
      xs[i] = __builtin_amdgcn_raw_buffer_load_b128(rx, lane * 16 + i * 1024, b * 8192 + w * 2048, 0);
    }
  }
  SB();
  const int cp = ((b >> 4) * 8 + (b & 7)) * 2 + ((b >> 3) & 1);
  const int c = w >> 1, kind = w & 1, d = cp * 2 + c;
  const int pi = d * N_ + 2 * lane;
  const u32 are = *(const u32*)(p.are + pi), aim = *(const u32*)(p.aim + pi);
  const u32 bre = *(const u32*)(p.bre + pi), bim = *(const u32*)(p.bim + pi), cre = *(const u32*)(p.cre + pi), cim = *(const u32*)(p.cim + pi);
  const u32 ldt = *(const u32*)(p.logdt + cp * 2), dsk = *(const u32*)(p.dskip + cp * 2);
  SB();
  SB();
  st16(rs, (tid < 56 && b * 7 + (tid >> 3) < CNT_LINES) ? tid * 16 : OOB, (u32)WS_CNT + b * 896, u32x4{0, 0, 0, 0});
  st16(rs, tid < 8 ? tid * 16 : OOB, (u32)WS_STAT + 32768 + b * 128, u32x4{0, 0, 0, 0});
  if (tid < 16) *(LAS u32x4*)(sm + M_KC + (tid >> 1) * KCS + 4096 + (tid & 1) * 16) = u32x4{0, 0, 0, 0};
  if (tid < 2) *(LAS u32*)(sm + P_T + tid * 1296 + 1288) = 0u;
  SB();
  const u32 tag = __builtin_amdgcn_readfirstlane(epoch) * 0x9E3779B1u + 0x7F4A7C15u;
  st16(rs, tid < 16 ? (tid >> 3) * 32768 + (tid & 7) * 16 : OOB, (u32)WS_TAGS + b * 128, u32x4{0u, tag, 0u, 0u});
  {
    const u32 vo = (((b & 1) * 64 + (tid >> 3) * 2) * L_ + (b >> 1) * 16 + 2 * (tid & 7)) * 8 + (u32)WS_XQ0;
    st16(rs, vo, 0, u32x4{xin[0].x, xin[0].y, xin[1].x, xin[1].y});
    st16(rs, vo, L_ * 8, u32x4{xin[0].z, xin[0].w, xin[1].z, xin[1].w});
    float st[4];
#pragma unroll
    for (int i = 0; i < 2; i++) {
      f32x2 s1 = {0.f, 0.f}, s2 = s1;
#pragma unroll
      for (int e = 0; e < 4; e++) { const f32x2 x = unp(xs[i][e]); s1 += x; s2 += x * x; }
      st[i] = s1.x + s1.y; st[2 + i] = s2.x + s2.y;
    }
    auto a0 = __builtin_amdgcn_permlane16_swap(__float_as_uint(st[0]), __float_as_uint(st[2]), false, false);
    auto a1 = __builtin_amdgcn_permlane16_swap(__float_as_uint(st[1]), __float_as_uint(st[3]), false, false);
    float v0 = __uint_as_float(a0[0]) + __uint_as_float(a0[1]), v1 = __uint_as_float(a1[0]) + __uint_as_float(a1[1]);
    auto a2 = __builtin_amdgcn_permlane32_swap(__float_as_uint(v0), __float_as_uint(v1), false, false);
    float v = __uint_as_float(a2[0]) + __uint_as_float(a2[1]);
    DPP_ADD(v, 0x128); DPP_ADD(v, 0x124); DPP_ADD(v, 0x122); DPP_ADD(v, 0x121);
    st8(rs, r == 0 ? (g4 & 1) * 16384 + (g4 >> 1) * 8 : OOB, (u32)WS_STAT + b * 64 + w * 16, __builtin_bit_cast(u32x2, (double)v));
  }
  SB();
  SB();
  const float S = 16384.f, LOG2E = 1.44269504088896341f;
  LAS char* Wt = sm + P_WT + c * (32 * 528);
  LAS char* Vt = sm + P_VT + c * (32 * 528);
  const float dt = rbf(expf(c ? bf_hi(ldt) : bf_lo(ldt)));
  f32x2 zl = unp(are) * (dt * LOG2E);
  const f32x2 zt = unp(aim) * dt * 0.159154943091895336f;
  cx2 cc = cmul(cx2{unp(cre), unp(cim)}, cx2{unp(bre) * dt, unp(bim) * dt});
  float mu = fmaxf(zl.x, zl.y);
  float cm = fmaxf(fmaxf(fmaxf(fabsf(cc.re.x), fabsf(cc.re.y)), fmaxf(fabsf(cc.im.x), fabsf(cc.im.y))), 1e-30f);
  wave_max2(mu, cm);
  zl -= mu;
#define PUT(dst, row, v) *(LAS u32x2*)((dst) + (row) * 528 + lane * 8) = u32x2{pkh((v).re), pkh((v).im)}
  const cx2 st8c = cexp2(zl, -zt, 8.f, 0.f);
  cx2 eb = cexp2(zl, -zt, kind ? 192.f : 64.f, 14.f);
  cx2 ec = cexp2(zl, kind ? zt : -zt, kind ? 256.f : 128.f, kind ? 0.f : 14.f);
  const cx2 e1 = cexp2(zl, zt, 1.f, 0.f);
  const float dk = c ? bf_hi(dsk) : bf_lo(dsk);
  const float bound = cm * 182.f * __builtin_amdgcn_exp2f(fminf(fmaxf(mu * 2048.f, 0.f), 60.f)) + fabsf(dk);
  const float osc = 32768.f * __builtin_amdgcn_rcpf(bound);
  const float kinv = __builtin_amdgcn_rcpf(osc);
  pin(eb.re); pin(eb.im); pin(ec.re); pin(ec.im);
  asm volatile("" ::"v"(code_pf), "v"(e1.re), "v"(e1.im), "v"(st8c.re), "v"(st8c.im), "v"(kinv));
  wait_v<0>();
  BARX();
  const u32x2 ar1 = tc_arrive(rs, 0, b, tid);
  SB();
  wt_load(gw, wv);
  const u32x4 tt0 = *(const u32x4*)(&g_tabs.t[kind * 64 + lane][0]), tt1 = *(const u32x4*)(&g_tabs.t[kind * 64 + lane][4]);
  const u32x4 gt0 = *(const u32x4*)(&g_tabs.g[tid][0]);
  const u32x2 gt1 = *(const u32x2*)(&g_tabs.g[tid][8]);
  SB();
  if (kind == 0) {
    cx2 ea = cx2{f32x2{S, S}, f32x2{0.f, 0.f}};
#pragma unroll
    for (int j = 0; j < 8; j++) {
      PUT(Vt, j, ea); PUT(Vt, 8 + j, eb); PUT(Vt, 16 + j, ec);
      if (j < 7) { ea = cmul(ea, st8c); eb = cmul(eb, st8c); ec = cmul(ec, st8c); }
    }
  } else {
    const float sc = S * __builtin_amdgcn_rcpf(cm);
    cc.re *= sc; cc.im *= sc;
    const cx2 e2 = cmul(e1, e1), e4 = cmul(e2, e2);
    cx2 c1 = cmul(cc, ec), c2 = c1;
#pragma unroll
    for (int j = 0; j < 8; j++) {
      PUT(Wt, j, cc);
      PUT(Vt, 24 + j, eb);
      if (j < 4) PUT(Wt, 8 + j, c1);
      if (j < 7) { cc = cmul(cc, e1); c2 = cmul(c2, ec); eb = cmul(eb, st8c); }
      if (j < 3) c1 = cmul(c1, e2);
      if (j < 2) { PUT(Wt, 12 + 2 * j, c2); const cx2 d1 = cmul(c2, e4); PUT(Wt, 13 + 2 * j, d1); }
      else if (j < 7) PUT(Wt, 14 + j, c2);
    }
  }
#undef PUT
  tc_arrive_check(rs, 0, b, tid, ar1, tag);
  wt_proc(gw, wv, csv);
#pragma unroll
  for (int i = 0; i < 5; i++) wt_store(gw, wv, csv, i);
  const bool extra = (b & 0xf1) == 0xe1 && w == 0;
  const int xt = 1024 + ((b >> 1) & 7);
  u32x4 wv2[4];
  if (extra) wt_load(xt, wv2);
  BAR();
  const u32x2 p1 = tc_poll(rs, 0, b, tid);
  {
    LAS char* T = sm + P_T + c * 1296;
    LAS char* pa = Wt + r * 528 + (g4 & 1) * 256 + (g4 >> 1) * 128;
    LAS char* pb = Vt + (16 * kind + r) * 528 + (g4 & 1) * 256 + (g4 >> 1) * 128;
    u32x4 fa[2][8], fb[8];
#pragma unroll
    for (int s = 0; s < 8; s++) { fb[s] = *(LAS u32x4*)(pb + s * 16); fa[0][s] = *(LAS u32x4*)(pa + s * 16); fa[1][s] = *(LAS u32x4*)(pa + 16 * 528 + s * 16); }
    float fac[8];
    const float f0 = cm * (1.f / (S * S)) * osc;
#pragma unroll
    for (int j = 0; j < 8; j++) fac[j] = __builtin_amdgcn_exp2f(mu * (float)((j < 4 ? tt0[j & 3] : tt1[j & 3]) >> 16)) * f0;
    SB();
    f32x4 acc0 = {0.f, 0.f, 0.f, 0.f}, acc1 = acc0;
#pragma unroll
    for (int s = 0; s < 8; s++) { acc0 = mfma_f16(fa[0][s], fb[s], acc0); acc1 = mfma_f16(fa[1][s], fb[s], acc1); }
    const float add0 = tt0[0] == 0u ? dk * osc : 0.f;
#pragma unroll
    for (int j = 0; j < 4; j++) {
      *(LAS _Float16*)(T + (tt0[j] & 0xffffu)) = (_Float16)(acc0[j] * fac[j] + (j == 0 ? add0 : 0.f));
      *(LAS _Float16*)(T + (tt1[j] & 0xffffu)) = (_Float16)(acc1[j] * fac[4 + j]);
    }
  }
  tc_poll_check(rs, 0, b, tid, p1, tag);
  wait_v<0>();
  BAR();
  const u32x2 ar2 = tc_arrive(rs, 1, b, tid);
  u32x4 in_su[2], in_sq[2], in_xv[2];
#pragma unroll
  for (int j = 0; j < 2; j++) {
    const u32 t = 2 * (tid + 256 * j);
    in_su[j] = ld16(rs, t * 8, (u32)WS_STAT);
    in_sq[j] = ld16(rs, t * 8, (u32)WS_STAT + 16384);
    in_xv[j] = ld16(rs, t * 8, (u32)WS_XQ0 + (cp >> 1) * L_ * 8);
  }
  SB();
  sfor<2>([&](auto C2) {
    constexpr int c2 = C2;
    u32 wq[6];
#pragma unroll
    for (int e = 0; e < 6; e++) {
      const u32 gi = e < 4 ? gt0[e & 3] : gt1[e & 1];
      LAS char* T = sm + P_T + c2 * 1296;
      wq[e] = __builtin_bit_cast(u32, f16x2{*(LAS _Float16*)(T + (gi & 0xffffu)), *(LAS _Float16*)(T + (gi >> 16))});
    }
    const u32 a0 = __builtin_amdgcn_alignbit(wq[1], wq[0], 16), a1 = __builtin_amdgcn_alignbit(wq[2], wq[1], 16), a2 = __builtin_amdgcn_alignbit(wq[3], wq[2], 16),
              a3 = __builtin_amdgcn_alignbit(wq[4], wq[3], 16), a4 = __builtin_amdgcn_alignbit(wq[5], wq[4], 16);
    LAS char* dst = sm + M_KC + c2 * (4 * KCS) + tid * 16;
    sfor<4>([&](auto I) {
      constexpr int i = I;
      *(LAS u32x4*)(dst + i * KCS) = i == 0 ? u32x4{wq[0], wq[1], wq[2], wq[3]} : i == 1 ? u32x4{a0, a1, a2, a3} : i == 2 ? u32x4{wq[1], wq[2], wq[3], wq[4]} : u32x4{a1, a2, a3, a4};
      SB();
    });
  });
  if (extra) {
    wt_proc(xt, wv2, csv);
#pragma unroll
    for (int i = 0; i < 4; i++) wt_store(xt, wv2, csv, i);
  }
  tc_arrive_check(rs, 1, b, tid, ar2, tag);
  const u32x2 p2 = tc_poll(rs, 1, b, tid);
  if ((b & 1) * 8 + (b >> 5) == 15) main_body<true>(sm, ws, p.out, tag, kinv, in_su, in_sq, in_xv, p2);
  else main_body<false>(sm, ws, p.out, tag, kinv, in_su, in_sq, in_xv, p2);
  if (b == 0 && tid == 0) __builtin_amdgcn_raw_buffer_store_b32(tag, rs, 0, (u32)WS_EPOCH, AUX_SC1);
}

extern "C" void kernel_launch(void* const* d_in, const int* in_sizes, int n_in, void* d_out, int out_size, void* d_ws, size_t ws_size,
                              hipStream_t stream) {
  (void)hipFuncSetAttribute((const void*)k_main, hipFuncAttributeMaxDynamicSharedMemorySize, M_LDS);
  P0 p;
  p.x0 = (const u16*)d_in[0]; p.are = (const u16*)d_in[1]; p.aim = (const u16*)d_in[2]; p.bre = (const u16*)d_in[3]; p.bim = (const u16*)d_in[4];
  p.cre = (const u16*)d_in[5]; p.cim = (const u16*)d_in[6]; p.logdt = (const u16*)d_in[7]; p.dskip = (const u16*)d_in[8];
  p.wg = (const u16*)d_in[10]; p.wu = (const u16*)d_in[11]; p.wd = (const u16*)d_in[12]; p.ws = (char*)d_ws; p.out = (u16*)d_out;
  k_main<<<256, 256, M_LDS, stream>>>(p);
}
```

```cpp
#include <hip/hip_runtime.h>
#include <stdint.h>

typedef unsigned short u16;
typedef unsigned int u32;
typedef unsigned long long u64;
typedef float f32x2 __attribute__((ext_vector_type(2)));
typedef float f32x4 __attribute__((ext_vector_type(4)));
typedef double f64x2 __attribute__((ext_vector_type(2)));
typedef u32 u32x2 __attribute__((ext_vector_type(2)));
typedef u32 u32x4 __attribute__((ext_vector_type(4)));
typedef short s16x4 __attribute__((ext_vector_type(4)));
typedef __bf16 bf16x2 __attribute__((ext_vector_type(2)));
typedef __bf16 bf16x8 __attribute__((ext_vector_type(8)));
typedef _Float16 f16x2 __attribute__((ext_vector_type(2)));
typedef _Float16 f16x8 __attribute__((ext_vector_type(8)));
#define LAS __attribute__((address_space(3)))
#define DEVI __device__ __forceinline__

constexpr int L_ = 2048, D_ = 512, N_ = 128, F_ = 1376;
constexpr int NT1 = 172;
constexpr int KS2 = 43;

constexpr size_t WS_CSP = 0;
constexpr size_t WS_EPOCH = 1u << 20;
constexpr size_t WS_TAGS = WS_EPOCH + 65536;
constexpr size_t WS_STAT = (2u << 20) + 4096;
constexpr size_t WS_GRAN = WS_STAT + 2 * 32768;
constexpr size_t WS_XQ0 = WS_GRAN + 2 * 16384 + 65536;
constexpr size_t WS_XQ2 = WS_XQ0 + (2u << 20);
constexpr size_t WS_XT1 = WS_XQ2 + (2u << 20);
constexpr size_t WS_W1P = WS_XT1 + (4u << 20);
constexpr size_t WS_W2P = WS_W1P + (size_t)64 * 16 * 3 * 1024;
constexpr size_t WS_H = WS_W2P + (size_t)8 * KS2 * 4 * 1024;
constexpr size_t H_BYTES = (size_t)128 * NT1 * 256;
constexpr size_t WS_CNT = WS_H + 2 * H_BYTES;
constexpr int CNT_LOC = 6 * 256;
constexpr int CNT_LINES = CNT_LOC + 64;
constexpr size_t WS_END = WS_CNT + CNT_LINES * 128;

DEVI float bf_lo(u32 v) { return __uint_as_float(v << 16); }
DEVI float bf_hi(u32 v) { return __uint_as_float(v & 0xffff0000u); }
DEVI u32 pk_bf16(float a, float b) { f32x2 v = {a, b}; return __builtin_bit_cast(u32, __builtin_convertvector(v, bf16x2)); }
DEVI float rbf(float a) { return bf_lo(pk_bf16(a, a)); }
DEVI f32x4 mfma_bf16(u32x4 a, u32x4 b, f32x4 c) {
  return __builtin_amdgcn_mfma_f32_16x16x32_bf16(__builtin_bit_cast(bf16x8, a), __builtin_bit_cast(bf16x8, b), c, 0, 0, 0);
}
DEVI f32x4 mfma_f16(u32x4 a, u32x4 b, f32x4 c) {
  return __builtin_amdgcn_mfma_f32_16x16x32_f16(__builtin_bit_cast(f16x8, a), __builtin_bit_cast(f16x8, b), c, 0, 0, 0);
}
DEVI u32x2 tr_read(LAS char* p) { return __builtin_bit_cast(u32x2, __builtin_amdgcn_ds_read_tr16_b64_v4i16((LAS s16x4*)p)); }
DEVI float vmax(float a, float b) { float d; asm("v_max_f32 %0, %1, %2" : "=v"(d) : "v"(a), "v"(b)); return d; }
DEVI void wave_max2(float& x, float& y) {
  auto a = __builtin_amdgcn_permlane32_swap(__float_as_uint(x), __float_as_uint(y), false, false);
  float v = vmax(__uint_as_float(a[0]), __uint_as_float(a[1]));
#define DPP_MAX(ctrl) v = vmax(v, __builtin_bit_cast(float, __builtin_amdgcn_update_dpp(0, __builtin_bit_cast(int, v), ctrl, 0xf, 0xf, true)))
  DPP_MAX(0x128); DPP_MAX(0x124); DPP_MAX(0x122); DPP_MAX(0x121);
#undef DPP_MAX
  x = vmax(__builtin_bit_cast(float, __builtin_amdgcn_readlane(__builtin_bit_cast(int, v), 0)), __builtin_bit_cast(float, __builtin_amdgcn_readlane(__builtin_bit_cast(int, v), 16)));
  y = vmax(__builtin_bit_cast(float, __builtin_amdgcn_readlane(__builtin_bit_cast(int, v), 32)), __builtin_bit_cast(float, __builtin_amdgcn_readlane(__builtin_bit_cast(int, v), 48)));
}
DEVI f32x2 unp(u32 v) { return f32x2{bf_lo(v), bf_hi(v)}; }
DEVI u32 pkb(f32x2 v) { return __builtin_bit_cast(u32, __builtin_convertvector(v, bf16x2)); }
DEVI u32 pkh(f32x2 v) { return __builtin_bit_cast(u32, __builtin_convertvector(v, f16x2)); }
struct cx2 { f32x2 re, im; };
DEVI cx2 cmul(cx2 a, cx2 b) { return cx2{a.re * b.re - a.im * b.im, a.re * b.im + a.im * b.re}; }
DEVI cx2 cexp2(f32x2 zl, f32x2 zt, float m, float lg) {
  f32x2 t = zt * m, e = zl * m + lg;
  t -= f32x2{__builtin_rintf(t.x), __builtin_rintf(t.y)};
  e = f32x2{__builtin_amdgcn_exp2f(e.x), __builtin_amdgcn_exp2f(e.y)};
  return cx2{e * f32x2{__builtin_amdgcn_cosf(t.x), __builtin_amdgcn_cosf(t.y)}, e * f32x2{__builtin_amdgcn_sinf(t.x), __builtin_amdgcn_sinf(t.y)}};
}

struct P0 {
  const u16 *x0, *are, *aim, *bre, *bim, *cre, *cim, *logdt, *wg, *wu, *wd, *dskip;
  char* ws;
  u16* out;
};
constexpr int AUX_SC1 = 16;
typedef __amdgpu_buffer_rsrc_t rsrc_t;
DEVI u32x4 ld16(rsrc_t rs, u32 voff, u32 soff) { return __builtin_amdgcn_raw_buffer_load_b128(rs, voff, soff, AUX_SC1); }
DEVI void st16(rsrc_t rs, u32 voff, u32 soff, u32x4 v) { __builtin_amdgcn_raw_buffer_store_b128(v, rs, voff + soff, 0, AUX_SC1); }
DEVI void st8(rsrc_t rs, u32 voff, u32 soff, u32x2 v) { __builtin_amdgcn_raw_buffer_store_b64(v, rs, voff, soff, AUX_SC1); }
#define SB() __builtin_amdgcn_sched_barrier(0)
#define BAR() { SB(); asm volatile("s_waitcnt lgkmcnt(0)" ::: "memory"); __builtin_amdgcn_s_barrier(); asm volatile("" ::: "memory"); SB(); }
#define BARX() { SB(); asm volatile("" ::: "memory"); __builtin_amdgcn_s_barrier(); asm volatile("" ::: "memory"); SB(); }
template <int N> DEVI void wait_v() { asm volatile("s_waitcnt vmcnt(%0)" ::"n"(N) : "memory"); }
constexpr int SPIN_CAP = 1 << 18;
DEVI u32 opq(u32 x) { asm volatile("" : "+s"(x)); return x; }
template <int V> struct IC { static constexpr int v = V; constexpr operator int() const { return V; } };
template <int I, int N, class F> DEVI void sfor_(F&& f) { if constexpr (I < N) { f(IC<I>{}); sfor_<I + 1, N>(f); } }
template <int N, class F> DEVI void sfor(F&& f) { sfor_<0, N>(f); }

__device__ double buf_fadd_f64(double, __amdgpu_buffer_rsrc_t, int, int, int) __asm("llvm.amdgcn.raw.ptr.buffer.atomic.fadd.f64");
__device__ long buf_add_i64(long, __amdgpu_buffer_rsrc_t, int, int, int) __asm("llvm.amdgcn.raw.ptr.buffer.atomic.add.i64");
template <class T> DEVI void pin(T& x) { asm volatile("" : "+v"(x)); }
constexpr u32 OOB = 0x7f000000u;
template <bool Z> DEVI void mmA(f32x4& d, const u32x4& a, const u32x4& b) {
  if constexpr (Z) asm volatile("v_mfma_f32_16x16x32_bf16 %0, %1, %2, 0" : "=&v"(d) : "a"(a), "v"(b));
  else asm volatile("v_mfma_f32_16x16x32_bf16 %0, %1, %2, %0" : "+v"(d) : "a"(a), "v"(b));
}
DEVI void mmV(f32x4& d, const u32x4& a, const u32x4& b) { asm volatile("v_mfma_f32_16x16x32_bf16 %0, %1, %2, %0" : "+v"(d) : "v"(a), "v"(b)); }
DEVI void mmVz(f32x4& d, const u32x4& a, const u32x4& b) { asm volatile("v_mfma_f32_16x16x32_bf16 %0, %1, %2, 0" : "=&v"(d) : "v"(a), "v"(b)); }
template <bool Z> DEVI void mmH(f32x4& d, const u32x4& a, const u32x4& b) {
  if constexpr (Z) asm volatile("v_mfma_f32_16x16x32_f16 %0, %1, %2, 0" : "=&v"(d) : "v"(a), "v"(b));
  else asm volatile("v_mfma_f32_16x16x32_f16 %0, %1, %2, %0" : "+v"(d) : "v"(a), "v"(b));
}
#define DPP_ADD(v, ctrl) v += __builtin_bit_cast(float, __builtin_amdgcn_update_dpp(0, __builtin_bit_cast(int, v), ctrl, 0xf, 0xf, true))
#define MM_DONE() { asm volatile("s_nop 7" ::: "memory"); SB(); }

DEVI void sig_all(rsrc_t rs, int ev, int b, int tid, bool en) {
  __builtin_amdgcn_raw_ptr_buffer_atomic_add_i32(1, rs, tid < (en ? 16 : 0) ? tid * 2048 : OOB, (u32)WS_CNT + (ev * 256 + (b & 15)) * 128, 0);
  SB();
}
DEVI void sig_loc(rsrc_t rs, int ev, int tid, bool en) {
  __builtin_amdgcn_raw_ptr_buffer_atomic_add_i32(1, rs, tid < (en ? 1 : 0) ? 0 : OOB, (u32)WS_CNT + (CNT_LOC + ev) * 128, 0);
  SB();
}
DEVI u32 po_all(int ev, int b) { return (u32)WS_CNT + (ev * 256 + (b & 15) * 16) * 128; }
DEVI u32 po_loc(int ev) { return (u32)WS_CNT + (CNT_LOC + ev) * 128; }
DEVI u32 poll_issue(rsrc_t rs, u32 vo, u32 so) {
  asm volatile("" ::: "memory");
  return (u32)__builtin_amdgcn_raw_ptr_buffer_atomic_add_i32(0, rs, vo, so, 0);
}
DEVI void poll_check(rsrc_t rs, u32 vo, u32 so, u32 v, u32 need) {
  pin(v);
  if (__builtin_expect(!__all(v >= need), 0)) {
#pragma unroll 1
    for (int spin = 0; spin < SPIN_CAP; spin++) {
      if (__all(poll_issue(rs, vo, so) >= need)) break;
      __builtin_amdgcn_s_sleep(2);
    }
  }
  asm volatile("" ::: "memory");
}

DEVI u32x2 tc_add(rsrc_t rs, u32 vo, u32 so, long v) {
  asm volatile("" ::: "memory");
  return __builtin_bit_cast(u32x2, buf_add_i64(v, rs, vo, so, 0));
}
DEVI u32x2 tc_arrive(rsrc_t rs, int ev, int b, int tid) { return tc_add(rs, tid < 16 ? tid * 2048 : OOB, (u32)WS_TAGS + (ev * 256 + (b & 15)) * 128, 1); }
DEVI void tc_arrive_check(rsrc_t rs, int ev, int b, int tid, u32x2 v, u32 tag) {
  pin(v);
  bool todo = tid < 16 && v.y != tag;
  if (__builtin_expect(__any(todo), 0)) {
#pragma unroll 1
    for (int spin = 0; spin < SPIN_CAP; spin++) {
      __builtin_amdgcn_s_sleep(2);
      v = tc_add(rs, todo ? tid * 2048 : OOB, (u32)WS_TAGS + (ev * 256 + (b & 15)) * 128, 1);
      todo = todo && v.y != tag;
      if (!__any(todo)) break;
    }
  }
}
DEVI u32x2 tc_poll(rsrc_t rs, int ev, int b, int tid) { return tc_add(rs, tid < 16 ? tid * 128 : OOB, (u32)WS_TAGS + (ev * 256 + (b & 15) * 16) * 128, 0); }
DEVI void tc_poll_check(rsrc_t rs, int ev, int b, int tid, u32x2 v, u32 tag) {
  pin(v);
  if (__builtin_expect(__any(tid < 16 && (v.y != tag || v.x < 16u)), 0)) {
#pragma unroll 1
    for (int spin = 0; spin < SPIN_CAP; spin++) {
      v = tc_poll(rs, ev, b, tid);
      if (!__any(tid < 16 && (v.y != tag || v.x < 16u))) break;
      __builtin_amdgcn_s_sleep(2);
    }
  }
  asm volatile("" ::: "memory");
}

constexpr int KCS = 4128;
constexpr int M_KC = 0;
constexpr int M_W = 8 * KCS;
constexpr int M_U = M_W;
constexpr int M_XS = M_U + 2 * 4608;
constexpr int M_CRED = M_XS + 8192;
constexpr int M_X = M_W + 15 * 4096;
constexpr int M_H = M_W;
constexpr int M_RES = M_KC + 2560;
constexpr int M_LDS = M_W + 31 * 4096;

template <int LOFF, int GOFF> DEVI void gset(u32& so, u32 lbase, u32 gbase) {
  asm volatile("s_add_u32 m0, %1, %3\n\ts_add_u32 %0, %2, %4" : "=&s"(so) : "s"(lbase), "s"(gbase), "n"(LOFF), "n"(GOFF) : "memory", "scc");
}
template <int E> DEVI void gld(u32 voff, rsrc_t rs, u32 so) {
  asm volatile("buffer_load_dwordx4 %0, %1, %2 offen offset:%3 sc1 lds" ::"v"(voff), "s"(rs), "s"(so), "n"(E * 1024) : "memory");
}

template <bool STATS>
DEVI void main_body(LAS char* sm, char* __restrict__ ws, u16* __restrict__ out, const u32 GTAG, const float kinv,
                    u32x4 (&in_su)[2], u32x4 (&in_sq)[2], u32x4 (&in_xv)[2], u32x2 t2v) {
  u32x4 wf[48];
  f32x4 cs[3];
  const int w = __builtin_amdgcn_readfirstlane(threadIdx.x >> 6);
#define TIDS() int tid = threadIdx.x; asm volatile("" : "+v"(tid)); const int lane = tid & 63, r = lane & 15, g4 = lane >> 4; (void)r; (void)g4;
  const int b = blockIdx.x, xcd = b & 7, idx = b >> 3;
  const int cp = ((b >> 4) * 8 + (b & 7)) * 2 + ((b >> 3) & 1);
  const int tt = (xcd >> 1) * 4 + (idx & 3), chunk = (xcd & 1) * 8 + (idx >> 2);
  const int tb = (xcd >> 1) * 8 + (idx & 7), db = (xcd & 1) * 4 + (idx >> 3);
  const int grp = chunk * 4 + w;
  const rsrc_t rs = __builtin_amdgcn_make_buffer_rsrc((void*)ws, 0, (int)WS_END, 0x00020000);
  const rsrc_t rs_out = __builtin_amdgcn_make_buffer_rsrc((void*)out, 0, L_ * D_ * 2, 0x00020000);
  auto conv_in1 = [&](int tid, int layer, int h, auto N) {
    constexpr int j = N / 3, e = N % 3;
    const u32 vo = tid * 16;
    if constexpr (e == 0) in_su[j] = ld16(rs, vo, (u32)WS_STAT + layer * 32768 + h * 8192 + j * 4096);
    if constexpr (e == 1) in_sq[j] = ld16(rs, vo, (u32)WS_STAT + layer * 32768 + h * 8192 + 16384 + j * 4096);
    if constexpr (e == 2) in_xv[j] = ld16(rs, vo, (u32)WS_XQ0 + layer * (2u << 20) + ((cp >> 1) * L_ + 1024 * h) * 8 + j * 4096);
  };
  u32x4 A0[4][4];
#define LDW1(n) if constexpr (!STATS) wf[n] = ld16(rs, lane * 16 + ((n) & 3) * 1024, o_w1 + ((n) >> 2) * 4096)
#define OW1() const u32 o_w1 = opq((u32)WS_W1P + grp * 49152);
      auto ln = [&](int tid, int h) {
        if (h == 0 && tid < 64) *(LAS u32x4*)(sm + M_U + (tid >> 5) * 4608 + (tid & 31) * 16) = u32x4{0, 0, 0, 0};
        f32x2 mean[2], var[2], ri[2];
#pragma unroll
        for (int j = 0; j < 2; j++) {
          const f64x2 s1 = __builtin_bit_cast(f64x2, in_su[j]), s2 = __builtin_bit_cast(f64x2, in_sq[j]);
          mean[j] = f32x2{(float)s1.x, (float)s1.y} * (1.f / D_);
          var[j] = f32x2{(float)s2.x, (float)s2.y} * (1.f / D_) - mean[j] * mean[j];
        }
#pragma unroll
        for (int j = 0; j < 2; j++) var[j] = unp(pkb(f32x2{fmaxf(var[j].x, 0.f), fmaxf(var[j].y, 0.f)})) + 1e-5f;
#pragma unroll
        for (int j = 0; j < 2; j++) { var[j] = unp(pkb(var[j])); mean[j] = unp(pkb(mean[j])); }
#pragma unroll
        for (int j = 0; j < 2; j++) var[j] = f32x2{__builtin_amdgcn_sqrtf(var[j].x), __builtin_amdgcn_sqrtf(var[j].y)};
        u32 xc[2][2];
        f32x2 d[2][2];
#pragma unroll
        for (int j = 0; j < 2; j++) {
          const u32 p0 = in_xv[j].x, p1 = in_xv[j].y, p2 = in_xv[j].z, p3 = in_xv[j].w;
          const u32 xa = (cp & 1) ? p1 : p0, xb = (cp & 1) ? p3 : p2;
          xc[j][0] = __builtin_amdgcn_perm(xb, xa, 0x05040100u);
          xc[j][1] = __builtin_amdgcn_perm(xb, xa, 0x07060302u);
          d[j][0] = f32x2{bf_lo(xa), bf_lo(xb)} - mean[j];
          d[j][1] = f32x2{bf_hi(xa), bf_hi(xb)} - mean[j];
        }
#pragma unroll
        for (int j = 0; j < 2; j++) { var[j] = unp(pkb(var[j])); ri[j] = f32x2{__builtin_amdgcn_rcpf(var[j].x), __builtin_amdgcn_rcpf(var[j].y)}; }
#pragma unroll
        for (int j = 0; j < 2; j++)
#pragma unroll
          for (int cc = 0; cc < 2; cc++) {
            *(LAS u32*)(sm + M_XS + h * 4096 + cc * 2048 + (tid + 256 * j) * 4) = xc[j][cc];
            *(LAS u32*)(sm + M_U + cc * 4608 + 512 + 2048 * h + (tid + 256 * j) * 4) = pkh(d[j][cc] * ri[j]);
          }
      };
  {
    TIDS()
    OW1()
    ln(tid, 0);
    tc_poll_check(rs, 1, b, tid, t2v, GTAG);
    BAR();
    const int cpy = (3 - r) & 3;
    LAS char* pa = sm + M_KC + (w >> 1) * (4 * KCS) + cpy * KCS + 2 * (15 + 8 * g4 - r - cpy) + 64 * (4 - 4 * (w & 1)) + 2048;
    sfor<16>([&](auto n) {
      constexpr int rr = n / 4, i = n % 4;
      const u32x2 lo = *(LAS u32x2*)(pa + 512 * (3 - i) + 64 * (3 - rr)), hi = *(LAS u32x2*)(pa + 512 * (3 - i) + 64 * (3 - rr) + 8);
      A0[rr][i] = u32x4{lo.x, lo.y, hi.x, hi.y};
      if constexpr (n < 15) { LDW1(n); }
      if constexpr (!STATS && n % 2 == 1 && n < 6) cs[n / 2] = __builtin_bit_cast(f32x4, ld16(rs, r * 256 + g4 * 16, (u32)WS_CSP + ((grp * 3 + n / 2) >> 2) * 4096 + ((grp * 3 + n / 2) & 3) * 64));
      SB();
    });
  }
#pragma unroll 1
  for (int layer = 0; layer < 2; layer++) {
    const u32 o_xt = opq((u32)WS_XT1 + layer * (2u << 20));
    const u32 o_h = opq((u32)WS_H + layer * (u32)H_BYTES);
    const u32 o_gr = (u32)WS_GRAN + layer * 16384;
    u32x4 A2[43];
    const u32 o_w2 = opq((u32)WS_W2P + (db * 4 + w) * KS2 * 1024);
    auto ldw2 = [&](int lane, auto n) { A2[n] = ld16(rs, lane * 16 + (n & 3) * 1024, o_w2 + (n >> 2) * 4096); };
#define LDWC(n) if constexpr (STATS) ldw2(lane, IC<(n) - 15>{}); else wf[n] = ld16(rs, lane * 16 + ((n) & 3) * 1024, o_w1 + ((n) >> 2) * 4096)
    u32 gv0, gso;
    const u32 ldsw = opq((u32)(uintptr_t)sm + w * 4096);
    const u32 o_xs = opq(o_xt + tt * 65536 + w * 4096);
    auto stage = [&](auto H, auto N) {
      if constexpr (N % 4 == 0) gset<M_X + (N / 4) * 16384, H * (1 << 20) + (N / 4) * 16384>(gso, ldsw, o_xs);
      gld<N % 4>(gv0, rs, gso);
    };
    {
      TIDS()
      OW1()
      gv0 = lane * 16;
      const int c = w >> 1, half = w & 1;
      const int cpy = (3 - r) & 3;
      LAS char* pa = sm + M_KC + c * (4 * KCS) + cpy * KCS + 2 * (15 + 8 * g4 - r - cpy) + 64 * (4 - 4 * half);
      LAS char* pb = sm + M_U + c * 4608 + 2 * (240 + 16 * r + 8 * g4) - 64 * (4 * half + 3);
      u32x4 A1[4][4], B[4][4];
      f32x4 acc[4], accH[4];
      auto rdA = [&](int rr, int i) {
        const u32x2 lo = *(LAS u32x2*)(pa + 512 * (3 - i) + 64 * (3 - rr)), hi = *(LAS u32x2*)(pa + 512 * (3 - i) + 64 * (3 - rr) + 8);
        A1[rr][i] = u32x4{lo.x, lo.y, hi.x, hi.y};
      };
      LAS char* cred = sm + M_CRED + w * 4096 + lane * 16;
      auto tri = [&](auto H, f32x4 (&ac)[4], auto side) {
        constexpr int h = H;
        sfor<16>([&](auto n) { B[n / 4][n % 4] = *(LAS u32x4*)(pb + 2048 * h + 512 * (n % 4) + 64 * (3 - n / 4)); SB(); });
        sfor<40>([&](auto n) {
          constexpr int g = n < 16 ? 3 : n < 28 ? 2 : n < 36 ? 1 : 0, e = n - (g == 3 ? 0 : g == 2 ? 16 : g == 1 ? 28 : 36);
          constexpr int rr = e / (g + 1), m = e % (g + 1);
          mmH<(h == 0 && e == 0)>(ac[g], A0[rr][g - m], B[rr][m]);
          if constexpr (n == 24) *(LAS f32x4*)(cred + 3072) = ac[3];
          if constexpr (n == 36) *(LAS f32x4*)(cred + 2048) = ac[2];
          side(n);
          SB();
        });
      };
      f32x4 kk[2], rv[2];
      u32x2 xr[2];
      u32 o[2][2];
      auto red_wr = [&](f32x4 (&ac)[4], int g) { *(LAS f32x4*)(cred + g * 1024) = ac[g]; };
      auto epi_rd = [&](int h, int g) {
        LAS char* p0 = sm + M_CRED + (w & 2) * 4096 + half * 2048 + lane * 16 + g * 1024;
        kk[g] = *(LAS f32x4*)p0;
        rv[g] = *(LAS f32x4*)(p0 + 4096);
        xr[g] = *(LAS u32x2*)(sm + M_XS + h * 4096 + c * 2048 + (256 * (2 * half + g) + 16 * r + 4 * g4) * 2);
      };
      auto epi_c = [&](int g, int e) {
        const f32x2 v = e ? f32x2{kk[g].z, kk[g].w} + f32x2{rv[g].z, rv[g].w} : f32x2{kk[g].x, kk[g].y} + f32x2{rv[g].x, rv[g].y};
        o[g][e] = pkb(v * kinv + unp(e ? xr[g].y : xr[g].x));
      };
      const int dch = cp * 2 + c, fd = ((dch >> 1) & 1) | (((dch >> 3) & 1) << 1);
      const u32 vo_st = (4 * (2 * half + (g4 & 1)) + (r >> 2)) * 65536 + ((r & 3) ^ fd) * 32 + (g4 & 2) * 8 + o_xt + dch * 128;
      auto epi_st = [&](int h) {
        auto e0 = __builtin_amdgcn_permlane16_swap(o[0][0], o[1][0], false, false);
        auto e1 = __builtin_amdgcn_permlane16_swap(o[0][1], o[1][1], false, false);
        st16(rs, vo_st, h << 20, u32x4{e0[0], e1[0], e0[1], e1[1]});
      };
      const u32 pvo = tid < 16 ? tid * 128 : OOB;
      const u32 n32 = tid < 16 ? 16 : 0;
      u32 pv;
      { u32 a_ = (u32)(uintptr_t)pa, b_ = (u32)(uintptr_t)pb; pin(a_); pin(b_); pa = (LAS char*)(uintptr_t)a_; pb = (LAS char*)(uintptr_t)b_; pin(gv0); }
      BAR();
      tri(IC<0>{}, acc, [&](auto n) {
        if constexpr (n % 8 == 3) { LDWC(15 + n / 8); }
        if constexpr (n >= 24 && n % 4 == 1) rdA(0, (n - 24) / 4);
        if constexpr (n == 14) { wait_v<(14 + 5) / 8>(); BARX(); sig_all(rs, 3, b, tid, layer); }
      });
      sfor<64>([&](auto n) {
        constexpr int rr = n / 16, m = 3 - (n % 16) / 4, g = n % 4, s = n / 4, ia = 4 + g - m;
        mmH<(n < 4)>(accH[g], ia < 4 ? A0[rr][ia & 3] : A1[rr][ia & 3], B[rr][m]);
        if constexpr (n == 3) red_wr(acc, 1);
        if constexpr (n == 6) red_wr(acc, 0);
        if constexpr (n == 10) { BAR(); epi_rd(0, 0); epi_rd(0, 1); }
        if constexpr (n >= 15 && n < 23 && n % 2 == 1) epi_c((n - 15) / 4, ((n - 15) / 2) & 1);
        if constexpr (n == 23) epi_st(0);
        if constexpr (n % 4 == 0 && rr < 3 && n > 0) rdA((n - 4) / 16 + 1, (s - 1) % 4);
        if constexpr (n == 48) rdA(3, 3);
        if constexpr (n >= 24 && n % 5 == 1 && n < 46) { LDWC(20 + (n - 26) / 5); }
        if constexpr (n == 2) pv = poll_issue(rs, pvo, po_all(3, b));
        if constexpr (n == 46) { poll_check(rs, pvo, po_all(3, b), pv, layer ? n32 : 0); BARX(); }
        if constexpr (n > 46 && (n - 46) % 2 == 1 && (n - 46) / 2 < 6) conv_in1(tid, layer, 1, IC<(n - 46) / 2>{});
        SB();
      });
      static_assert(46 + 11 < 64 && 46 >= 42 && 2 < 23, "");
      sfor<4>([&](auto n) { LDWC(24 + n); });
      wait_v<14>();
      BARX();
      sig_all(rs, layer * 4, b, tid, true);
      sfor<4>([&](auto n) { LDWC(28 + n); });
      SB();
      ln(tid, 1);
      BAR();
      tri(IC<1>{}, accH, [&](auto n) {
        if constexpr (n % 6 == 3 && n < 24) { LDWC(32 + n / 6); }
        if constexpr (n == 4) pv = poll_issue(rs, pvo, po_all(layer * 4, b));
      });
      asm volatile("s_nop 3");
      red_wr(accH, 1);
      asm volatile("s_nop 3");
      SB();
      red_wr(accH, 0);
      poll_check(rs, pvo, po_all(layer * 4, b), pv, n32);
      BAR();
      sfor<2>([&](auto k) { stage(IC<0>{}, k); });
      epi_rd(1, 0); epi_rd(1, 1);
      sfor<2>([&](auto k) { stage(IC<0>{}, IC<2 + k>{}); });
      SB();
      epi_c(0, 0); epi_c(0, 1); epi_c(1, 0); epi_c(1, 1);
      epi_st(1);
      SB();
      sfor<4>([&](auto k) { stage(IC<0>{}, IC<4 + k>{}); });
    }
    u32x4 res;
    f32x4 ac2[4];
    u32x2 rx[2];
    auto res_a = [&](int lane) {
      LAS char* rb = sm + M_RES + w * KCS;
      *(LAS u32x4*)(rb + (lane >> 2) * 80 + (lane & 3) * 16) = res;
#pragma unroll
      for (int i = 0; i < 2; i++) rx[i] = tr_read(rb + (lane >> 2) * 80 + i * 32 + (lane & 3) * 8);
    };
    auto res_b = [&]() {
#pragma unroll
      for (int i = 0; i < 2; i++) { ac2[i] = f32x4{bf_lo(rx[i].x), bf_hi(rx[i].x), bf_lo(rx[i].y), bf_hi(rx[i].y)}; pin(ac2[i]); }
      SB();
    };
    const u32 o_hr = opq(o_h + (tb * 2 + (w & 1)) * (NT1 * 256) + (w >> 1) * 4096);
    auto sth = [&](auto H, auto N) {
      if constexpr (N % 4 == 0) gset<M_H + (N / 4) * 16384, H * 64 * (NT1 * 256) + (N / 4) * 8192>(gso, ldsw, o_hr);
      gld<N % 4>(gv0, rs, gso);
    };
    auto ldres = [&](int tid, int h) {
      const int dl = tid >> 2, fd = ((dl >> 1) & 1) | (((dl >> 3) & 1) << 1);
      res = ld16(rs, dl * 128 + ((2 * (tb & 1) + ((tid >> 1) & 1)) ^ fd) * 32 + (tid & 1) * 16, o_xt + (h << 20) + (tb >> 1) * 65536 + db * 8192);
    };
    {
      TIDS()
      const int q = r >> 2, pp = lane & 3;
      const int fsw = ((q >> 1) & 1) | ((g4 & 1) << 1);
      const u32 pvo = tid < 16 ? tid * 128 : OOB, pv0 = tid == 0 ? 0 : OOB, n32 = tid < 16 ? 16 : 0, n15 = tid == 0 ? 15 : 0;
      const u32 pah = po_all(layer * 4 + 1, b), pbl = po_loc((layer * 2) * 16 + (tb >> 1));
      if constexpr (STATS) {
        sfor<8>([&](auto k) { stage(IC<0>{}, IC<8 + k>{}); });
        LAS char* xr = sm + M_X + (8 * g4 + q) * 128 + pp * 8 + 32 * (w ^ fsw);
        u32 pv;
        sfor<2>([&](auto H) {
          constexpr int h = H;
          const u32x4 ones = {0x3f803f80u, 0x3f803f80u, 0x3f803f80u, 0x3f803f80u};
          f32x4 as = {0.f, 0.f, 0.f, 0.f}, aq = as;
          sfor<2>([&](auto KK) {
            wait_v<KK == 0 ? 8 : h == 0 ? 1 : 0>();
            BARX();
            if constexpr (h == 0 && KK == 0) sig_all(rs, layer * 4 + 1, b, tid, true);
            u32x2 lo[8], hi[8];
#pragma unroll
            for (int k = 0; k < 8; k++) { lo[k] = tr_read(xr + (8 * KK + k) * 4096); hi[k] = tr_read(xr + (8 * KK + k) * 4096 + 512); }
            SB();
#pragma unroll
            for (int k = 0; k < 8; k++) {
              const u32x4 fr = {lo[k].x, lo[k].y, hi[k].x, hi[k].y};
              as = mfma_bf16(ones, fr, as);
              aq = mfma_bf16(fr, fr, aq);
            }
            pin(as); pin(aq);
          });
          const float ssq = pp == 0 ? aq[0] : pp == 1 ? aq[1] : pp == 2 ? aq[2] : aq[3];
          const float mean = as[0] * (1.f / D_);
          const float var = fmaxf(ssq * (1.f / D_) - mean * mean, 0.f);
          const float sd = __builtin_amdgcn_sqrtf(rbf(rbf(var) + 1e-5f));
          st8(rs, q == g4 ? (w * 16 + r) * 8 : OOB, o_gr + (1024 * h + tt * 64) * 8, u32x2{pk_bf16(mean, sd), GTAG});
          SB();
          if constexpr (h == 0) {
            sfor<10>([&](auto n) { ldw2(lane, IC<21 + n>{}); });
            SB();
            pv = poll_issue(rs, pvo, pah);
            poll_check(rs, pvo, pah, pv, n32);
            BAR();
            sfor<16>([&](auto k) { stage(IC<1>{}, k); });
          }
        });
        sfor<7>([&](auto n) { ldw2(lane, IC<31 + n>{}); });
        ldres(tid, 0);
        pv = poll_issue(rs, pv0, pbl);
        poll_check(rs, pv0, pbl, pv, n15);
        res_a(lane);
        BAR();
        sfor<23>([&](auto j) { sth(IC<0>{}, j); });
      } else {
        f32x4 acc[4][3];
        u32x2 gr[4];
        u32 hvo[3];
#pragma unroll
        for (int nn = 0; nn < 3; nn++) {
          const int item = 4 * nn + g4, ii = item / 3, jj = item - 3 * ii;
          hvo[nn] = ii * (NT1 * 256) + jj * 256 + r * 16 + ((grp * 3 + jj < NT1) ? 0u : OOB) + o_h + tt * 4 * (NT1 * 256) + grp * 768;
          pin(hvo[nn]);
        }
        u32 pv;
        auto kloop = [&](auto H, auto side) {
          constexpr int h = H;
          LAS char* xr = sm + M_X + (8 * g4 + q) * 128 + pp * 8;
          u32x2 lo[2][4], hi[2][4];
          sfor<4>([&](auto i) { lo[0][i] = tr_read(xr + 32 * (i ^ fsw)); hi[0][i] = tr_read(xr + 32 * (i ^ fsw) + 512); SB(); });
          sfor<64>([&](auto n) {
            constexpr int k = n / 4, i = n % 4;
            const u32x4 xf = {lo[k & 1][i].x, lo[k & 1][i].y, hi[k & 1][i].x, hi[k & 1][i].y};
            side(IC<2 * n>{});
            SB();
            mmA<k == 0>(acc[i][0], wf[k * 3], xf);
            if constexpr (k < 15) lo[(k + 1) & 1][i] = tr_read(xr + (k + 1) * 4096 + 32 * (i ^ fsw));
            SB();
            mmA<k == 0>(acc[i][1], wf[k * 3 + 1], xf);
            if constexpr (k < 15) hi[(k + 1) & 1][i] = tr_read(xr + (k + 1) * 4096 + 32 * (i ^ fsw) + 512);
            SB();
            mmA<k == 0>(acc[i][2], wf[k * 3 + 2], xf);
            side(IC<2 * n + 1>{});
            SB();
          });
          MM_DONE();
        };
        auto ldgr = [&](int h, int i) { gr[i] = __builtin_amdgcn_raw_buffer_load_b64(rs, r * 8 + i * 128, o_gr + (1024 * h + tt * 64) * 8, AUX_SC1); };
        auto epi1 = [&](int h, auto side) {
#define GR_OK() __all((gr[0].y == GTAG) & (gr[1].y == GTAG) & (gr[2].y == GTAG) & (gr[3].y == GTAG))
          if (__builtin_expect(!GR_OK(), 0)) {
#pragma unroll 1
            for (int spin = 0; spin < SPIN_CAP; spin++) {
              asm volatile("" ::: "memory");
              ldgr(h, 0); ldgr(h, 1); ldgr(h, 2); ldgr(h, 3);
              if (GR_OK()) break;
            }
          }
          u32 hv[12];
          sfor<4>([&](auto I) {
            constexpr int i = I;
            const float m = bf_lo(gr[i].x), sd = bf_hi(gr[i].x);
            const float kx = -1.44269504088896341f * __builtin_amdgcn_rcpf(sd), cd = -1.44269504088896341f * sd;
            f32x2 y[3], au[3], e[3];
#pragma unroll
            for (int j = 0; j < 3; j++) y[j] = f32x2{acc[i][j].x, acc[i][j].y} - m * f32x2{cs[j].x, cs[j].y};
#pragma unroll
            for (int j = 0; j < 3; j++) { y[j] *= kx; au[j] = f32x2{acc[i][j].z, acc[i][j].w} - m * f32x2{cs[j].z, cs[j].w}; }
#pragma unroll
            for (int j = 0; j < 3; j++) e[j] = f32x2{__builtin_amdgcn_exp2f(y[j].x), __builtin_amdgcn_exp2f(y[j].y)};
            side(IC<3 * i>{});
#pragma unroll
            for (int j = 0; j < 3; j++) { e[j] = e[j] * cd + cd; y[j] *= au[j]; }
            side(IC<3 * i + 1>{});
#pragma unroll
            for (int j = 0; j < 3; j++) e[j] = f32x2{__builtin_amdgcn_rcpf(e[j].x), __builtin_amdgcn_rcpf(e[j].y)};
            side(IC<3 * i + 2>{});
#pragma unroll
            for (int j = 0; j < 3; j++) hv[3 * i + j] = pkb(y[j] * e[j]);
            if constexpr (i >= 1) {
              constexpr int nn = i - 1;
              auto s02 = __builtin_amdgcn_permlane32_swap(hv[4 * nn], hv[4 * nn + 2], false, false);
              auto s13 = __builtin_amdgcn_permlane32_swap(hv[4 * nn + 1], hv[4 * nn + 3], false, false);
              auto a01 = __builtin_amdgcn_permlane16_swap(s02[0], s13[0], false, false);
              auto a23 = __builtin_amdgcn_permlane16_swap(s02[1], s13[1], false, false);
              st16(rs, hvo[nn], h * 64 * (NT1 * 256), u32x4{a01[0], a01[1], a23[0], a23[1]});
            }
            SB();
          });
        };
        OW1()
        if (layer == 0) {
#pragma unroll
          for (int j = 0; j < 3; j++)
#pragma unroll
            for (int e = 0; e < 4; e++) { float v = cs[j][e]; DPP_ADD(v, 0x128); DPP_ADD(v, 0x124); DPP_ADD(v, 0x122); DPP_ADD(v, 0x121); cs[j][e] = v; }
        }
        wait_v<5>();
        BARX();
        kloop(IC<0>{}, [&](auto n2) {
          constexpr int k = n2 / 8, i = (n2 / 2) % 4, post = n2 & 1;
          if constexpr (!post && i == 0 && k == 3) { wait_v<9>(); BARX(); sig_all(rs, layer * 4 + 1, b, tid, true); }
          if constexpr (!post && i == 0 && k == 7) { wait_v<10>(); BARX(); }
          if constexpr (!post && i == 0 && k == 11) { wait_v<7>(); BARX(); }
          if constexpr (post && i == 0 && k < 8) stage(IC<0>{}, IC<8 + k>{});
          if constexpr (post && (i & 1) && k < 6) { LDW1(36 + 2 * k + (i >> 1)); }
          if constexpr (post && i == 2 && k >= 6 && k < 14) ldw2(lane, IC<(k < 8 ? k - 6 : 2 * k - 14)>{});
          if constexpr (post && i == 0 && k >= 8 && k < 14) ldw2(lane, IC<2 * k - 13>{});
          if constexpr (post && k == 13 && (i & 1)) { ldgr(0, 2 * (i >> 1)); ldgr(0, 2 * (i >> 1) + 1); }
          if constexpr (post && i == 1 && k == 11) pv = poll_issue(rs, pvo, pah);
        });
        poll_check(rs, pvo, pah, pv, n32);
        BARX();
        epi1(0, [&](auto n) {
          if constexpr (n < 4) { stage(IC<1>{}, IC<2 * n>{}); stage(IC<1>{}, IC<2 * n + 1>{}); }
          else stage(IC<1>{}, IC<4 + n>{});
        });
        wait_v<15>();
        BAR();
        kloop(IC<1>{}, [&](auto n2) {
          constexpr int k = n2 / 8, i = (n2 / 2) % 4, post = n2 & 1;
          if constexpr (!post && i == 0 && k == 3) { wait_v<9 + 4>(); BARX(); }
          if constexpr (!post && i == 0 && k == 5) { wait_v<8>(); BARX(); sig_loc(rs, (layer * 2) * 16 + tt, tid, true); }
          if constexpr (post && !(i & 1) && k >= 1 && k < 13) ldw2(lane, IC<12 + 2 * k + (i >> 1)>{});
          if constexpr (post && i == 1 && k == 10) pv = poll_issue(rs, pv0, pbl);
          if constexpr (post && k == 10 && (i & 1)) { ldgr(1, 2 * (i >> 1)); ldgr(1, 2 * (i >> 1) + 1); }
          if constexpr (post && i == 3 && k == 9) ldres(tid, 0);
          if constexpr (!post && i == 0 && k == 14) { pin(gr[3]); poll_check(rs, pv0, pbl, pv, n15); BARX(); }
          if constexpr (post && i < 3 && k >= 14) sth(IC<0>{}, IC<3 * (k - 14) + i>{});
          if constexpr (post && i == 3 && k == 15) res_a(lane);
        });
        BARX();
        epi1(1, [&](auto n) {
          if constexpr (n < 5) { sth(IC<0>{}, IC<6 + 2 * n>{}); sth(IC<0>{}, IC<7 + 2 * n>{}); }
          else sth(IC<0>{}, IC<11 + n>{});
        });
      }
    }
    {
      TIDS()
      OW1()
      auto mm2 = [&](auto side) {
        constexpr int PD = 8;
        u32x4 Bf[10];
        LAS char* hb = sm + M_H + lane * 16;
        LAS char* hb2 = hb + 32768;
        auto rdh = [&](auto n) {
          constexpr int s = n / 2, o = (s >> 3) * 16384 + (((s >> 2) & 1) * 2 + (n & 1)) * 4096 + (s & 3) * 1024;
          Bf[n % 10] = o < 32768 ? *(LAS u32x4*)(hb + o) : *(LAS u32x4*)(hb2 + (o - 32768));
        };
        sfor<PD>([&](auto n) { rdh(n); SB(); });
        sfor<86>([&](auto n) {
          constexpr int m = n ^ 1;
          side(IC<2 * n>{});
          SB();
          if constexpr (m == 2 || m == 3) mmVz(ac2[m], A2[1], Bf[m]);
          else if constexpr (m / 2 < 10) mmV(ac2[m & 3], A2[m / 2], Bf[m % 10]);
          else mmA<false>(ac2[m & 3], A2[m / 2], Bf[m % 10]);
          if constexpr (n + PD < 86) rdh(IC<n + PD>{});
          side(IC<2 * n + 1>{});
          SB();
        });
        MM_DONE();
      };
      const u32 ovo = r * (layer ? D_ * 2 : 8) + g4 * (layer ? 8 : L_ * 8);
      auto epi2 = [&](int h) {
        float s1[2], s2[2];
#pragma unroll
        for (int i = 0; i < 2; i++) {
          const f32x4 v = ac2[i] + ac2[2 + i];
          const u32 o0 = pkb(f32x2{v.x, v.y}), o1 = pkb(f32x2{v.z, v.w});
          st8(rs, layer ? OOB : ovo, (u32)WS_XQ2 + ((db * 16 + 4 * w) * L_ + 1024 * h + tb * 32 + 16 * i) * 8, u32x2{o0, o1});
          __builtin_amdgcn_raw_buffer_store_b64(u32x2{o0, o1}, rs_out, layer ? ovo : OOB, ((1024 * h + tb * 32 + 16 * i) * D_ + db * 64 + 16 * w) * 2, 0);
          const float a = bf_lo(o0), bq = bf_hi(o0), cq = bf_lo(o1), dq = bf_hi(o1);
          s1[i] = (a + bq) + (cq + dq);
          s2[i] = (a * a + bq * bq) + (cq * cq + dq * dq);
        }
        auto x0 = __builtin_amdgcn_permlane16_swap(__float_as_uint(s1[0]), __float_as_uint(s2[0]), false, false);
        auto x1 = __builtin_amdgcn_permlane16_swap(__float_as_uint(s1[1]), __float_as_uint(s2[1]), false, false);
        const float t0 = __uint_as_float(x0[0]) + __uint_as_float(x0[1]), t1 = __uint_as_float(x1[0]) + __uint_as_float(x1[1]);
        auto x2 = __builtin_amdgcn_permlane32_swap(__float_as_uint(t0), __float_as_uint(t1), false, false);
        const float t2 = __uint_as_float(x2[0]) + __uint_as_float(x2[1]);
        buf_fadd_f64((double)t2, rs, layer ? OOB : (g4 & 1) * 16384 + ((g4 >> 1) * 16 + r) * 8, (u32)WS_STAT + 32768 + (1024 * h + tb * 32) * 8, 0);
        SB();
      };
      const u32 nxt = layer ? OOB : 0u;
      const u32 pbh = po_loc((layer * 2 + 1) * 16 + (tb >> 1)), pvo = tid < 16 ? tid * 128 : OOB, pv0 = tid == 0 ? 0 : OOB;
      u32 pv;
      res_b();
      wait_v<STATS ? 15 : 18>();
      BARX();
      mm2([&](auto n2) {
        constexpr int n = n2 / 2, post = n2 & 1, j = n / 4;
        if constexpr (post && n % 4 == 1 && j < 5) ldw2(lane, IC<38 + j>{});
        if constexpr (post && n % 4 == 1 && j == 5) ldres(tid, 1);
        if constexpr (!STATS && post && n % 4 == 1 && j >= 6 && j < 21) wf[j - 6] = ld16(rs, (lane * 16 | nxt) + ((j - 6) & 3) * 1024, o_w1 + ((j - 6) >> 2) * 4096);
        if constexpr (!post && n == 24) { wait_v<STATS ? 7 + 6 : 10 + 6>(); BARX(); }
        if constexpr (!post && n == 48) { wait_v<STATS ? 6 : 48 / 4>(); BARX(); if constexpr (!STATS) sig_loc(rs, (layer * 2 + 1) * 16 + tt, tid, true); }
        if constexpr (post && n == 85) pv = poll_issue(rs, pv0, pbh);
        if constexpr (post && n == 79) res_a(lane);
      });
      epi2(0);
      res_b();
      poll_check(rs, pv0, pbh, pv, tid == 0 ? 15 : 0);
      BARX();
      sfor<12>([&](auto j) { sth(IC<1>{}, j); });
      wait_v<8>();
      BARX();
      sig_all(rs, 2, b, tid, !layer);
      mm2([&](auto n2) {
        constexpr int n = n2 / 2, post = n2 & 1, j = n / 4;
        if constexpr (post && n % 4 == 1 && j + 12 < 23) sth(IC<1>{}, IC<12 + j>{});
        if constexpr (!post && n % 16 == 8 && n < 80) {
          constexpr int J = (n + 8) / 16, issued = 12 + j < 23 ? 12 + j : 23, need = J == 4 ? 23 : 4 * J + 4;
          if constexpr (J < 5) wait_v<issued - need>();
          if constexpr (J == 5) poll_check(rs, pvo, po_all(2, b), pv, (tid < 16 && !layer) ? 16 : 0);
          BARX();
        }
        if constexpr (post && n == 34) pv = poll_issue(rs, layer ? OOB : pvo, po_all(2, b));
        if constexpr (post && n >= 73 && n % 2 == 1 && n < 85) conv_in1(tid | (nxt >> 4), 1, 0, IC<(n - 73) / 2>{});
      });
      epi2(1);
      pin(in_xv[1]);
      if (layer) break;
      ln(tid, 0);
      SB();
    }
  }
}

constexpr int P_WT = M_W;
constexpr int P_VT = P_WT + 2 * 32 * 528;
constexpr int P_T = P_VT + 2 * 32 * 528;
constexpr int P_TR = P_T + 2 * 648 * 2;
static_assert(P_TR + 4 * 32 * 144 <= M_LDS && P_TR % 16 == 0, "");

constexpr int c_rne(int l) {
  int sh = 0;
  while ((l >> sh) >= 256) sh++;
  if (sh == 0) return l;
  const int q = l >> sh, rem = l & ((1 << sh) - 1), half = 1 << (sh - 1);
  return (q + ((rem > half || (rem == half && (q & 1))) ? 1 : 0)) << sh;
}
constexpr int c_idx(int lam) { int sh = 0; while ((lam >> sh) >= 256) sh++; return sh == 0 ? lam : 128 * sh + (lam >> sh); }
struct Tabs { u16 g[256][16]; u32 t[128][8]; };
constexpr Tabs make_tabs() {
  Tabs x{};
  for (int t = 0; t < 256; t++)
    for (int e = 0; e < 16; e++) { const int l = 2047 - (8 * t + e); x.g[t][e] = (u16)(2 * (l < 0 ? 645 : c_idx(c_rne(l)))); }
  for (int i = 0; i < 128; i++)
    for (int j = 0; j < 8; j++) {
      const int col = 16 * (i >> 6) + (i & 15), row = 16 * (j >> 2) + 4 * ((i >> 4) & 3) + (j & 3);
      const int al = row < 8 ? row : row < 12 ? 256 + 2 * (row - 8) : row < 16 ? 512 + 256 * ((row - 12) >> 1) + 4 * (row & 1) : 1024 + 256 * (row - 16);
      const int lam = al + 8 * col;
      const bool ok = row < 20 || (row == 20 && col == 0);
      x.t[i][j] = ok ? ((u32)lam << 16) | (u32)(2 * c_idx(lam)) : (u32)(2 * 644) | (1u << 16);
    }
  return x;
}
__device__ const Tabs g_tabs __attribute__((aligned(128))) = make_tabs();

__global__ void __launch_bounds__(256, 1) k_main(P0 p) {
  extern __shared__ __attribute__((aligned(16))) char smem_[];
  LAS char* sm = (LAS char*)smem_;
  char* ws = p.ws;
  const int b = blockIdx.x, tid = threadIdx.x, lane = tid & 63, w = __builtin_amdgcn_readfirstlane(tid >> 6);
  const int r = lane & 15, g4 = lane >> 4;
  const rsrc_t rs = __builtin_amdgcn_make_buffer_rsrc((void*)ws, 0, (int)WS_END, 0x00020000);
  constexpr int CODE_BYTES = 46080;
  const u32 code_pf = __builtin_amdgcn_raw_buffer_load_b32(
      __builtin_amdgcn_make_buffer_rsrc((void*)(__builtin_amdgcn_s_getpc() & ~127ull), 0, CODE_BYTES, 0x00020000),
      tid < 12 ? ((b >> 3) * 12 + tid) * 128 : OOB, 0, 0);
  SB();
  auto wt_load = [&](int task, u32x4 (&v)[4]) {
    const bool up = task < 688;
    const int t2 = task - 688;
    const u32 so = up ? ((task & 15) * 32 * F_ + (task >> 4) * 32) * 2 : ((t2 >> 3) * 32 * D_ + (t2 & 7) * 64) * 2;
    const u32 ld = up ? F_ * 2 : D_ * 2;
    const rsrc_t r0 = __builtin_amdgcn_make_buffer_rsrc((void*)(up ? p.wg : p.wd), 0, D_ * F_ * 2, 0x00020000);
    const rsrc_t r1 = __builtin_amdgcn_make_buffer_rsrc((void*)(up ? p.wu : p.wd), 0, D_ * F_ * 2, 0x00020000);
    const u32 vo = (lane >> 2) * ld + (lane & 3) * 16, s1 = so + (up ? 0 : 64);
    v[0] = __builtin_amdgcn_raw_buffer_load_b128(r0, vo, so, 0); v[1] = __builtin_amdgcn_raw_buffer_load_b128(r1, vo, s1, 0);
    v[2] = __builtin_amdgcn_raw_buffer_load_b128(r0, vo, so + 16 * ld, 0); v[3] = __builtin_amdgcn_raw_buffer_load_b128(r1, vo, s1 + 16 * ld, 0);
  };
  auto wt_proc = [&](int task, u32x4 (&v)[4], u32x4& csv) {
    LAS char* tr = sm + P_TR + w * (32 * 144);
    if (task < 688) {
#pragma unroll
      for (int i = 0; i < 2; i++) {
        LAS char* dst = tr + (i * 16 + (lane >> 2)) * 144 + (lane & 3) * 32;
        *(LAS u32x4*)dst = u32x4{v[2 * i].x, v[2 * i + 1].x, v[2 * i].y, v[2 * i + 1].y};
        *(LAS u32x4*)(dst + 16) = u32x4{v[2 * i].z, v[2 * i + 1].z, v[2 * i].w, v[2 * i + 1].w};
      }
    } else {
#pragma unroll
      for (int i = 0; i < 4; i++) *(LAS u32x4*)(tr + ((i >> 1) * 16 + (lane >> 2)) * 144 + (i & 1) * 64 + (lane & 3) * 16) = v[i];
    }
    f32x4 cs = {0.f, 0.f, 0.f, 0.f};
#pragma unroll
    for (int i = 0; i < 4; i++) {
      LAS char* a = tr + (8 * g4 + (r >> 2)) * 144 + i * 32 + (lane & 3) * 8;
      const u32x2 lo = tr_read(a), hi = tr_read(a + 4 * 144);
      v[i] = u32x4{lo.x, lo.y, hi.x, hi.y};
      const u32 one = r == i ? 0x3f803f80u : 0u;
      cs = mfma_bf16(v[i], u32x4{one, one, one, one}, cs);
    }
    csv = __builtin_bit_cast(u32x4, cs);
  };
  auto wt_store = [&](int task, u32x4 (&v)[4], const u32x4& csv, int i) {
    const bool up = task < 688;
    if (i == 4) {
      st16(rs, (r < 4 && up) ? r * 64 + g4 * 16 : OOB, (u32)WS_CSP + task * 256, csv);
    } else {
      const int t2 = task - 688, tw = (task >> 4) * 4 + i, g3 = (tw * 43691) >> 17;
      const u32 o1 = (u32)WS_W1P + (g3 * 45 + (task & 15) * 3 + tw) * 1024, o2 = (u32)WS_W2P + (((t2 & 7) * 4 + i) * KS2 + (t2 >> 3)) * 1024;
      st16(rs, lane * 16, up ? o1 : o2, v[i]);
    }
    SB();
  };
  const int gw0 = b * 4 + w, ksq = (gw0 * 1525) >> 16;
  const int gw = gw0 < 688 ? (gw0 - ksq * 43) * 16 + ksq : gw0;
  u32x4 wv[4], xin[2], xs[2], csv;
  const u32 epoch = __builtin_amdgcn_raw_buffer_load_b32(rs, 0, (u32)WS_EPOCH, AUX_SC1);
  {
    const rsrc_t rx = __builtin_amdgcn_make_buffer_rsrc((void*)p.x0, 0, L_ * D_ * 2, 0x00020000);
#pragma unroll
    for (int i = 0; i < 2; i++) {
      xin[i] = __builtin_amdgcn_raw_buffer_load_b128(rx, (tid & 7) * 2048 + (tid >> 3) * 16 + i * 1024, (b >> 1) * 16384 + (b & 1) * 512, 0);
      xs[i] = __builtin_amdgcn_raw_buffer_load_b128(rx, lane * 16 + i * 1024, b * 8192 + w * 2048, 0);
    }
  }
  SB();
  const int cp = ((b >> 4) * 8 + (b & 7)) * 2 + ((b >> 3) & 1);
  const int c = w >> 1, kind = w & 1, d = cp * 2 + c;
  const int pi = d * N_ + 2 * lane;
  const u32 are = *(const u32*)(p.are + pi), aim = *(const u32*)(p.aim + pi);
  const u32 bre = *(const u32*)(p.bre + pi), bim = *(const u32*)(p.bim + pi), cre = *(const u32*)(p.cre + pi), cim = *(const u32*)(p.cim + pi);
  const u32 ldt = *(const u32*)(p.logdt + cp * 2), dsk = *(const u32*)(p.dskip + cp * 2);
  SB();
  SB();
  st16(rs, (tid < 56 && b * 7 + (tid >> 3) < CNT_LINES) ? tid * 16 : OOB, (u32)WS_CNT + b * 896, u32x4{0, 0, 0, 0});
  st16(rs, tid < 8 ? tid * 16 : OOB, (u32)WS_STAT + 32768 + b * 128, u32x4{0, 0, 0, 0});
  if (tid < 16) *(LAS u32x4*)(sm + M_KC + (tid >> 1) * KCS + 4096 + (tid & 1) * 16) = u32x4{0, 0, 0, 0};
  if (tid < 2) *(LAS u32*)(sm + P_T + tid * 1296 + 1288) = 0u;
  SB();
  const u32 tag = __builtin_amdgcn_readfirstlane(epoch) * 0x9E3779B1u + 0x7F4A7C15u;
  st16(rs, tid < 16 ? (tid >> 3) * 32768 + (tid & 7) * 16 : OOB, (u32)WS_TAGS + b * 128, u32x4{0u, tag, 0u, 0u});
  {
    const u32 vo = (((b & 1) * 64 + (tid >> 3) * 2) * L_ + (b >> 1) * 16 + 2 * (tid & 7)) * 8 + (u32)WS_XQ0;
    st16(rs, vo, 0, u32x4{xin[0].x, xin[0].y, xin[1].x, xin[1].y});
    st16(rs, vo, L_ * 8, u32x4{xin[0].z, xin[0].w, xin[1].z, xin[1].w});
    float st[4];
#pragma unroll
    for (int i = 0; i < 2; i++) {
      f32x2 s1 = {0.f, 0.f}, s2 = s1;
#pragma unroll
      for (int e = 0; e < 4; e++) { const f32x2 x = unp(xs[i][e]); s1 += x; s2 += x * x; }
      st[i] = s1.x + s1.y; st[2 + i] = s2.x + s2.y;
    }
    auto a0 = __builtin_amdgcn_permlane16_swap(__float_as_uint(st[0]), __float_as_uint(st[2]), false, false);
    auto a1 = __builtin_amdgcn_permlane16_swap(__float_as_uint(st[1]), __float_as_uint(st[3]), false, false);
    float v0 = __uint_as_float(a0[0]) + __uint_as_float(a0[1]), v1 = __uint_as_float(a1[0]) + __uint_as_float(a1[1]);
    auto a2 = __builtin_amdgcn_permlane32_swap(__float_as_uint(v0), __float_as_uint(v1), false, false);
    float v = __uint_as_float(a2[0]) + __uint_as_float(a2[1]);
    DPP_ADD(v, 0x128); DPP_ADD(v, 0x124); DPP_ADD(v, 0x122); DPP_ADD(v, 0x121);
    st8(rs, r == 0 ? (g4 & 1) * 16384 + (g4 >> 1) * 8 : OOB, (u32)WS_STAT + b * 64 + w * 16, __builtin_bit_cast(u32x2, (double)v));
  }
  SB();
  SB();
  const float S = 16384.f, LOG2E = 1.44269504088896341f;
  LAS char* Wt = sm + P_WT + c * (32 * 528);
  LAS char* Vt = sm + P_VT + c * (32 * 528);
  const float dt = rbf(expf(c ? bf_hi(ldt) : bf_lo(ldt)));
  f32x2 zl = unp(are) * (dt * LOG2E);
  const f32x2 zt = unp(aim) * dt * 0.159154943091895336f;
  cx2 cc = cmul(cx2{unp(cre), unp(cim)}, cx2{unp(bre) * dt, unp(bim) * dt});
  float mu = fmaxf(zl.x, zl.y);
  float cm = fmaxf(fmaxf(fmaxf(fabsf(cc.re.x), fabsf(cc.re.y)), fmaxf(fabsf(cc.im.x), fabsf(cc.im.y))), 1e-30f);
  wave_max2(mu, cm);
  zl -= mu;
#define PUT(dst, row, v) *(LAS u32x2*)((dst) + (row) * 528 + lane * 8) = u32x2{pkh((v).re), pkh((v).im)}
  const cx2 st8c = cexp2(zl, -zt, 8.f, 0.f);
  cx2 eb = cexp2(zl, -zt, kind ? 192.f : 64.f, 14.f);
  cx2 ec = cexp2(zl, kind ? zt : -zt, kind ? 256.f : 128.f, kind ? 0.f : 14.f);
  const cx2 e1 = cexp2(zl, zt, 1.f, 0.f);
  const float dk = c ? bf_hi(dsk) : bf_lo(dsk);
  const float bound = cm * 182.f * __builtin_amdgcn_exp2f(fminf(fmaxf(mu * 2048.f, 0.f), 60.f)) + fabsf(dk);
  const float osc = 32768.f * __builtin_amdgcn_rcpf(bound);
  const float kinv = __builtin_amdgcn_rcpf(osc);
  pin(eb.re); pin(eb.im); pin(ec.re); pin(ec.im);
  asm volatile("" ::"v"(code_pf), "v"(e1.re), "v"(e1.im), "v"(st8c.re), "v"(st8c.im), "v"(kinv));
  wait_v<0>();
  BARX();
  const u32x2 ar1 = tc_arrive(rs, 0, b, tid);
  SB();
  wt_load(gw, wv);
  const u32x4 tt0 = *(const u32x4*)(&g_tabs.t[kind * 64 + lane][0]), tt1 = *(const u32x4*)(&g_tabs.t[kind * 64 + lane][4]);
  const u32x4 gt0 = *(const u32x4*)(&g_tabs.g[tid][0]);
  const u32x2 gt1 = *(const u32x2*)(&g_tabs.g[tid][8]);
  SB();
  if (kind == 0) {
    cx2 ea = cx2{f32x2{S, S}, f32x2{0.f, 0.f}};
#pragma unroll
    for (int j = 0; j < 8; j++) {
      PUT(Vt, j, ea); PUT(Vt, 8 + j, eb); PUT(Vt, 16 + j, ec);
      if (j < 7) { ea = cmul(ea, st8c); eb = cmul(eb, st8c); ec = cmul(ec, st8c); }
    }
  } else {
    const float sc = S * __builtin_amdgcn_rcpf(cm);
    cc.re *= sc; cc.im *= sc;
    const cx2 e2 = cmul(e1, e1), e4 = cmul(e2, e2);
    cx2 c1 = cmul(cc, ec), c2 = c1;
#pragma unroll
    for (int j = 0; j < 8; j++) {
      PUT(Wt, j, cc);
      PUT(Vt, 24 + j, eb);
      if (j < 4) PUT(Wt, 8 + j, c1);
      if (j < 7) { cc = cmul(cc, e1); c2 = cmul(c2, ec); eb = cmul(eb, st8c); }
      if (j < 3) c1 = cmul(c1, e2);
      if (j < 2) { PUT(Wt, 12 + 2 * j, c2); const cx2 d1 = cmul(c2, e4); PUT(Wt, 13 + 2 * j, d1); }
      else if (j < 7) PUT(Wt, 14 + j, c2);
    }
  }
#undef PUT
  tc_arrive_check(rs, 0, b, tid, ar1, tag);
  wt_proc(gw, wv, csv);
#pragma unroll
  for (int i = 0; i < 5; i++) wt_store(gw, wv, csv, i);
  const bool extra = (b & 0xf1) == 0xe1 && w == 0;
  const int xt = 1024 + ((b >> 1) & 7);
  u32x4 wv2[4];
  if (extra) wt_load(xt, wv2);
  BAR();
  const u32x2 p1 = tc_poll(rs, 0, b, tid);
  {
    LAS char* T = sm + P_T + c * 1296;
    LAS char* pa = Wt + r * 528 + (g4 & 1) * 256 + (g4 >> 1) * 128;
    LAS char* pb = Vt + (16 * kind + r) * 528 + (g4 & 1) * 256 + (g4 >> 1) * 128;
    u32x4 fa[2][8], fb[8];
#pragma unroll
    for (int s = 0; s < 8; s++) { fb[s] = *(LAS u32x4*)(pb + s * 16); fa[0][s] = *(LAS u32x4*)(pa + s * 16); fa[1][s] = *(LAS u32x4*)(pa + 16 * 528 + s * 16); }
    float fac[8];
    const float f0 = cm * (1.f / (S * S)) * osc;
#pragma unroll
    for (int j = 0; j < 8; j++) fac[j] = __builtin_amdgcn_exp2f(mu * (float)((j < 4 ? tt0[j & 3] : tt1[j & 3]) >> 16)) * f0;
    SB();
    f32x4 acc0 = {0.f, 0.f, 0.f, 0.f}, acc1 = acc0;
#pragma unroll
    for (int s = 0; s < 8; s++) { acc0 = mfma_f16(fa[0][s], fb[s], acc0); acc1 = mfma_f16(fa[1][s], fb[s], acc1); }
    const float add0 = tt0[0] == 0u ? dk * osc : 0.f;
#pragma unroll
    for (int j = 0; j < 4; j++) {
      *(LAS _Float16*)(T + (tt0[j] & 0xffffu)) = (_Float16)(acc0[j] * fac[j] + (j == 0 ? add0 : 0.f));
      *(LAS _Float16*)(T + (tt1[j] & 0xffffu)) = (_Float16)(acc1[j] * fac[4 + j]);
    }
  }
  tc_poll_check(rs, 0, b, tid, p1, tag);
  wait_v<0>();
  BAR();
  const u32x2 ar2 = tc_arrive(rs, 1, b, tid);
  u32x4 in_su[2], in_sq[2], in_xv[2];
#pragma unroll
  for (int j = 0; j < 2; j++) {
    const u32 t = 2 * (tid + 256 * j);
    in_su[j] = ld16(rs, t * 8, (u32)WS_STAT);
    in_sq[j] = ld16(rs, t * 8, (u32)WS_STAT + 16384);
    in_xv[j] = ld16(rs, t * 8, (u32)WS_XQ0 + (cp >> 1) * L_ * 8);
  }
  SB();
  sfor<2>([&](auto C2) {
    constexpr int c2 = C2;
    u32 wq[6];
#pragma unroll
    for (int e = 0; e < 6; e++) {
      const u32 gi = e < 4 ? gt0[e & 3] : gt1[e & 1];
      LAS char* T = sm + P_T + c2 * 1296;
      wq[e] = __builtin_bit_cast(u32, f16x2{*(LAS _Float16*)(T + (gi & 0xffffu)), *(LAS _Float16*)(T + (gi >> 16))});
    }
    const u32 a0 = __builtin_amdgcn_alignbit(wq[1], wq[0], 16), a1 = __builtin_amdgcn_alignbit(wq[2], wq[1], 16), a2 = __builtin_amdgcn_alignbit(wq[3], wq[2], 16),
              a3 = __builtin_amdgcn_alignbit(wq[4], wq[3], 16), a4 = __builtin_amdgcn_alignbit(wq[5], wq[4], 16);
    LAS char* dst = sm + M_KC + c2 * (4 * KCS) + tid * 16;
    sfor<4>([&](auto I) {
      constexpr int i = I;
      *(LAS u32x4*)(dst + i * KCS) = i == 0 ? u32x4{wq[0], wq[1], wq[2], wq[3]} : i == 1 ? u32x4{a0, a1, a2, a3} : i == 2 ? u32x4{wq[1], wq[2], wq[3], wq[4]} : u32x4{a1, a2, a3, a4};
      SB();
    });
  });
  if (extra) {
    wt_proc(xt, wv2, csv);
#pragma unroll
    for (int i = 0; i < 4; i++) wt_store(xt, wv2, csv, i);
  }
  tc_arrive_check(rs, 1, b, tid, ar2, tag);
  const u32x2 p2 = tc_poll(rs, 1, b, tid);
  if ((b & 1) * 8 + (b >> 5) == 15) main_body<true>(sm, ws, p.out, tag, kinv, in_su, in_sq, in_xv, p2);
  else main_body<false>(sm, ws, p.out, tag, kinv, in_su, in_sq, in_xv, p2);
  if (b == 0 && tid == 0) __builtin_amdgcn_raw_buffer_store_b32(tag, rs, 0, (u32)WS_EPOCH, AUX_SC1);
}

extern "C" void kernel_launch(void* const* d_in, const int* in_sizes, int n_in, void* d_out, int out_size, void* d_ws, size_t ws_size,
                              hipStream_t stream) {
  (void)hipFuncSetAttribute((const void*)k_main, hipFuncAttributeMaxDynamicSharedMemorySize, M_LDS);
  P0 p;
  p.x0 = (const u16*)d_in[0]; p.are = (const u16*)d_in[1]; p.aim = (const u16*)d_in[2]; p.bre = (const u16*)d_in[3]; p.bim = (const u16*)d_in[4];
  p.cre = (const u16*)d_in[5]; p.cim = (const u16*)d_in[6]; p.logdt = (const u16*)d_in[7]; p.dskip = (const u16*)d_in[8];
  p.wg = (const u16*)d_in[10]; p.wu = (const u16*)d_in[11]; p.wd = (const u16*)d_in[12]; p.ws = (char*)d_ws; p.out = (u16*)d_out;
  k_main<<<256, 256, M_LDS, stream>>>(p);
}
```
